```python
import torch

def _softmax(x, axis=-1):
    return torch.softmax(x, dim=axis)
def _gelu(x):
    return torch.nn.functional.gelu(x, approximate="tanh")
def _silu(x):
    return torch.nn.functional.silu(x)
def _sigmoid(x):
    return torch.sigmoid(x)
def _layernorm(x, axis=-1, eps=1e-5):
    m = x.mean(dim=axis, keepdim=True)
    v = x.var(dim=axis, keepdim=True, unbiased=False)
    return (x - m) / torch.sqrt(v + eps)
def _scatter_add(tgt, idx, src, axis=0):
    return tgt.clone().index_add_(axis, idx, src)
def _segment_sum(src, seg, n):
    out = torch.zeros((n,) + src.shape[1:], dtype=src.dtype)
    return out.index_add_(0, seg, src)
def _pad(x, pw):
    flat = tuple(v for pair in reversed(pw) for v in pair)
    return torch.nn.functional.pad(x, flat)

kernel_name = 'standalone__gqa_block_cached__near_ridge'

def setup_inputs():

    _w = (lambda a: (lambda t: t.to(torch.bfloat16) if t.is_floating_point() else t)(torch.as_tensor(a)))
    B, Sq, D = 8, 144, 640
    Hq, Hkv, Sk, Dh = 32, 32, 1024, 128
    assert Hq % Hkv == 0, (Hq, Hkv)
    return {
        "x": _w((lambda *s: torch.randn(*s, dtype=torch.bfloat16) * 0.8)(B, Sq, D)),
        "k_cache": _w((lambda *s: torch.randn(*s, dtype=torch.bfloat16) * 0.8)(B, Hkv, Sk, Dh)),
        "v_cache": _w((lambda *s: torch.randn(*s, dtype=torch.bfloat16) * 0.05)(B, Hkv, Sk, Dh)),
        "wq": _w((lambda *s: torch.randn(*s, dtype=torch.bfloat16) * 0.8)(D, Hq * Dh) * (D ** -0.5)),
        "wo": _w((lambda *s: torch.randn(*s, dtype=torch.bfloat16) * 0.05)(Hq * Dh, D) * ((Hq * Dh) ** -0.5)),
    }

def reference(x, k_cache, v_cache, wq, wo):
    B, Sq, D = x.shape
    Hq, Dh = 32, 128
    Hkv = k_cache.shape[1]
    rep = Hq // Hkv
    q = (lambda x, *d: x.permute(*d))((x @ wq).reshape(B, Sq, Hq, Dh), 0, 2, 1, 3)
    k = (lambda x, n, axis: torch.repeat_interleave(x, n, dim=axis))(k_cache, rep, axis=1)
    v = (lambda x, n, axis: torch.repeat_interleave(x, n, dim=axis))(v_cache, rep, axis=1)
    scores = torch.einsum("bhqd,bhkd->bhqk", q, k) * (Dh ** -0.5)
    p = _softmax(scores, axis=-1)
    o = torch.einsum("bhqk,bhkd->bhqd", p, v)
    return (lambda x, *d: x.permute(*d))(o, 0, 2, 1, 3).reshape(B, Sq, Hq * Dh) @ wo
```

```cpp
#include <hip/hip_runtime.h>
#include <hip/hip_bf16.h>
typedef unsigned short u16;
__device__ __forceinline__ float bf2f(u16 x){ return __uint_as_float(((unsigned)x)<<16); }
__device__ __forceinline__ u16 f2bf(float f){ return __bfloat16_as_ushort(__float2bfloat16(f)); }
constexpr int B=8,SQ=144,D=640,H=32,SK=1024,DH=128;
__global__ void gemm_naive(const u16* A,const u16* W,u16* C,int M,int N,int K){
  __shared__ float As[16][65]; __shared__ float Ws[64][65];
  int n0=blockIdx.x*64, m0=blockIdx.y*16; int t=threadIdx.x;
  int tn=t&63, tm=t>>6; float acc[4]={0,0,0,0};
  for(int k0=0;k0<K;k0+=64){
    for(int i=t;i<16*64;i+=256){int r=i>>6,c=i&63; As[r][c]=bf2f(A[(size_t)(m0+r)*K+k0+c]);}
    for(int i=t;i<64*64;i+=256){int r=i>>6,c=i&63; Ws[r][c]=bf2f(W[(size_t)(k0+r)*N+n0+c]);}
    __syncthreads();
    for(int k=0;k<64;k++){ float w=Ws[k][tn]; for(int j=0;j<4;j++) acc[j]+=As[tm*4+j][k]*w; }
    __syncthreads();
  }
  for(int j=0;j<4;j++) C[(size_t)(m0+tm*4+j)*N+n0+tn]=f2bf(acc[j]);
}
__global__ void attn_naive(const u16* Q,const u16* Kc,const u16* Vc,u16* O){
  __shared__ float p[4][SK]; __shared__ float qs[4][DH];
  int b=blockIdx.z,h=blockIdx.y; int w=threadIdx.x>>6,l=threadIdx.x&63; int q=blockIdx.x*4+w;
  const u16* qr=Q+((size_t)(b*SQ+q))*(H*DH)+h*DH;
  qs[w][l]=bf2f(qr[l]); qs[w][l+64]=bf2f(qr[l+64]);
  __syncthreads();
  const u16* Kh=Kc+((size_t)(b*H+h))*SK*DH; const u16* Vh=Vc+((size_t)(b*H+h))*SK*DH;
  float m=-1e30f;
  for(int k=l;k<SK;k+=64){ float s=0; for(int d=0;d<DH;d++) s+=qs[w][d]*bf2f(Kh[k*DH+d]); s*=0.08838834764831845f; p[w][k]=s; m=fmaxf(m,s);}
  for(int o=32;o;o>>=1) m=fmaxf(m,__shfl_xor(m,o));
  float sum=0; for(int k=l;k<SK;k+=64){ float e=__expf(p[w][k]-m); p[w][k]=e; sum+=e;}
  for(int o=32;o;o>>=1) sum+=__shfl_xor(sum,o);
  __syncthreads();
  float a0=0,a1=0; for(int k=0;k<SK;k++){ float e=p[w][k]; a0+=e*bf2f(Vh[k*DH+l]); a1+=e*bf2f(Vh[k*DH+l+64]); }
  u16* orow=O+((size_t)(b*SQ+q))*(H*DH)+h*DH; orow[l]=f2bf(a0/sum); orow[l+64]=f2bf(a1/sum);
}
extern "C" void kernel_launch(void* const* d_in,const int* in_sizes,int n_in,void* d_out,int out_size,void* d_ws,size_t ws_size,hipStream_t stream){
  const u16* x=(const u16*)d_in[0]; const u16* k=(const u16*)d_in[1]; const u16* v=(const u16*)d_in[2]; const u16* wq=(const u16*)d_in[3]; const u16* wo=(const u16*)d_in[4];
  u16* q=(u16*)d_ws; u16* o=q+(size_t)B*SQ*H*DH;
  gemm_naive<<<dim3(H*DH/64,B*SQ/16),256,0,stream>>>(x,wq,q,B*SQ,H*DH,D);
  attn_naive<<<dim3(SQ/4,H,B),256,0,stream>>>(q,k,v,o);
  gemm_naive<<<dim3(D/64,B*SQ/16),256,0,stream>>>(o,wo,(u16*)d_out,B*SQ,D,H*DH);
}
```

```cpp
#include <hip/hip_runtime.h>
#include <hip/hip_bf16.h>

typedef unsigned short u16;
typedef float f32x16 __attribute__((ext_vector_type(16)));
typedef float f32x4 __attribute__((ext_vector_type(4)));
typedef float f32x2 __attribute__((ext_vector_type(2)));
typedef __bf16 bf16x8 __attribute__((ext_vector_type(8)));
typedef __bf16 bf16x4 __attribute__((ext_vector_type(4)));
typedef __bf16 bf16x2 __attribute__((ext_vector_type(2)));
typedef unsigned u32x4 __attribute__((ext_vector_type(4)));
typedef unsigned u32x2 __attribute__((ext_vector_type(2)));
#define LAS __attribute__((address_space(3)))
#define DI __device__ __forceinline__

constexpr int NB = 8, SQ = 144, DM = 640, NH = 32, SK = 1024, DH = 128;
constexpr int HD = NH * DH;
constexpr float QSCALE = 0.12751743082f;
constexpr float THR = 8.0f;

#define STAMPS 0

constexpr unsigned KR = 0, VR = 65536;
constexpr unsigned WS0 = 32768, WS1 = 49152, WS2 = 90112;
constexpr unsigned XS0 = 106496, XSZ = 18432;
constexpr unsigned QX = 143360;
constexpr unsigned OX = 131072;
constexpr unsigned LDS_A = 163840;

DI unsigned rfl(unsigned x) { return __builtin_amdgcn_readfirstlane(x); }
DI unsigned cvtpk(float lo, float hi) { f32x2 v = {lo, hi}; bf16x2 b = __builtin_convertvector(v, bf16x2); return __builtin_bit_cast(unsigned, b); }
DI bf16x8 lds128(unsigned a) { return *(LAS bf16x8*)a; }
DI bf16x4 ldstr(unsigned a) { return __builtin_amdgcn_ds_read_tr16_b64_v4bf16((LAS bf16x4*)a); }
DI bf16x8 cat(bf16x4 a, bf16x4 b) { return __builtin_shufflevector(a, b, 0, 1, 2, 3, 4, 5, 6, 7); }
#define MFMA32(a, b, c) __builtin_amdgcn_mfma_f32_32x32x16_bf16((a), (b), (c), 0, 0, 0)
#define MFMA16(a, b, c) __builtin_amdgcn_mfma_f32_16x16x32_bf16((a), (b), (c), 0, 0, 0)
template <int N> DI void waitvm() { asm volatile("s_waitcnt vmcnt(%0)" ::"i"(N) : "memory"); }
DI void bar() { asm volatile("s_waitcnt lgkmcnt(0)\n\ts_barrier" ::: "memory"); }
DI void dma(unsigned m0, unsigned voff, __amdgpu_buffer_rsrc_t srd, unsigned soff) {
  asm volatile("s_mov_b32 m0, %0\n\ts_nop 0\n\tbuffer_load_dwordx4 %1, %2, %3 offen lds" ::"s"(m0), "v"(voff), "s"(srd), "s"(soff) : "memory");
}
DI void dma_nt(unsigned m0, unsigned voff, __amdgpu_buffer_rsrc_t srd, unsigned soff) {
  asm volatile("s_mov_b32 m0, %0\n\ts_nop 0\n\tbuffer_load_dwordx4 %1, %2, %3 offen nt lds" ::"s"(m0), "v"(voff), "s"(srd), "s"(soff) : "memory");
}
DI float swap32_max(float x) { auto r = __builtin_amdgcn_permlane32_swap(__float_as_uint(x), __float_as_uint(x), false, false); return fmaxf(__uint_as_float(r[0]), __uint_as_float(r[1])); }
DI float swap32_add(float x) { auto r = __builtin_amdgcn_permlane32_swap(__float_as_uint(x), __float_as_uint(x), false, false); return __uint_as_float(r[0]) + __uint_as_float(r[1]); }
DI float swap16_max(float x) { auto r = __builtin_amdgcn_permlane16_swap(__float_as_uint(x), __float_as_uint(x), false, false); return fmaxf(__uint_as_float(r[0]), __uint_as_float(r[1])); }
DI float swap16_add(float x) { auto r = __builtin_amdgcn_permlane16_swap(__float_as_uint(x), __float_as_uint(x), false, false); return __uint_as_float(r[0]) + __uint_as_float(r[1]); }
DI float max3f(float a, float b, float c) { return __builtin_fmaxf(__builtin_fmaxf(a, b), c); }

#define STAMP_DECL
#define CSTAMP(i) do {} while (0)
#define STAMP(i) do {} while (0)
#define STAMP_OUT(lo, hi) do {} while (0)

struct MainState {
  f32x16 O[4];
  f32x16 NEGM;
  bf16x8 Qf[8];
  bf16x8 Kf[8];
  float l;
  unsigned ka[8], va[4];
};

template <int SL> DI void main_iter(MainState& st, f32x16& Scur, f32x16& Snew, bool last) {
  bar();
  Snew = st.NEGM;
#pragma unroll
  for (int s = 0; s < 8; ++s) Snew = MFMA32(st.Kf[s], st.Qf[s], Snew);
  bf16x8 Vf[4][2];
#pragma unroll
  for (int ks = 0; ks < 2; ++ks)
#pragma unroll
    for (int dt = 0; dt < 4; ++dt) {
      const unsigned a = st.va[dt] + SL * 8192 + ks * 4096;
      Vf[dt][ks] = cat(ldstr(a), ldstr(a + 2048));
    }
#pragma unroll
  for (int r = 0; r < 16; ++r) Scur[r] = __builtin_amdgcn_exp2f(Scur[r]);
  bf16x8 Pk[2];
#pragma unroll
  for (int ks = 0; ks < 2; ++ks) {
    u32x4 w4;
#pragma unroll
    for (int e = 0; e < 4; ++e) w4[e] = cvtpk(Scur[8 * ks + 2 * e], Scur[8 * ks + 2 * e + 1]);
    Pk[ks] = __builtin_bit_cast(bf16x8, w4);
  }
#pragma unroll
  for (int i = 0; i < 8; ++i) {
    __builtin_amdgcn_sched_group_barrier(0x008, 1, 0);
    __builtin_amdgcn_sched_group_barrier(0x100, 2, 0);
    __builtin_amdgcn_sched_group_barrier(0x400, 2, 0);
    __builtin_amdgcn_sched_group_barrier(0x002, 1, 0);
  }
  __builtin_amdgcn_sched_barrier(0);
#pragma unroll
  for (int ks = 0; ks < 2; ++ks)
#pragma unroll
    for (int dt = 0; dt < 4; ++dt) st.O[dt] = MFMA32(Vf[dt][ks], Pk[ks], st.O[dt]);
#pragma unroll
  for (int s = 0; s < 8; ++s) st.Kf[s] = lds128(st.ka[s] + ((SL + 2) & 7) * 8192);
  float s0 = Scur[0] + Scur[1], s1 = Scur[2] + Scur[3];
#pragma unroll
  for (int r = 4; r < 16; r += 2) { s0 += Scur[r]; s1 += Scur[r + 1]; }
  st.l += s0 + s1;
  float m0 = max3f(Snew[0], Snew[1], Snew[2]), m1 = max3f(Snew[3], Snew[4], Snew[5]);
  m0 = max3f(m0, Snew[6], Snew[7]); m1 = max3f(m1, Snew[8], Snew[9]);
  m0 = max3f(m0, Snew[10], Snew[11]); m1 = max3f(m1, Snew[12], Snew[13]);
  m0 = max3f(m0, Snew[14], Snew[15]);
  float rmax = fmaxf(m0, m1);
#pragma unroll
  for (int i = 0; i < 8; ++i) {
    __builtin_amdgcn_sched_group_barrier(0x008, 1, 1);
    __builtin_amdgcn_sched_group_barrier(0x100, 1, 1);
    __builtin_amdgcn_sched_group_barrier(0x002, 4, 1);
  }
  __builtin_amdgcn_sched_barrier(0);
  if (__builtin_expect(__any(rmax > THR) && !last, 0)) {
    const float dl = fmaxf(swap32_max(rmax), 0.f);
    const float f = __builtin_amdgcn_exp2f(-dl);
#pragma unroll
    for (int dt = 0; dt < 4; ++dt)
#pragma unroll
      for (int r = 0; r < 16; ++r) st.O[dt][r] *= f;
    st.l *= f;
#pragma unroll
    for (int r = 0; r < 16; ++r) { Snew[r] -= dl; st.NEGM[r] -= dl; }
  }
}

__global__ __launch_bounds__(512, 2) void attn_kernel(const u16* __restrict__ x, const u16* __restrict__ kc, const u16* __restrict__ vc,
                                                      const u16* __restrict__ wq, u16* __restrict__ ot, unsigned long long* __restrict__ stamps) {
  extern __shared__ __attribute__((aligned(16))) char lds[];
  const unsigned ldsb = (unsigned)(size_t)(LAS char*)lds;
  const unsigned lane = threadIdx.x & 63u;
  const unsigned w = rfl(threadIdx.x >> 6);
  const unsigned bid = blockIdx.x, xg = bid & 7u, wi = bid >> 3;
  const unsigned h = (xg & 3u) * 8u + (wi & 7u), b = (xg >> 2) * 4u + (wi >> 3);
  STAMP_DECL;
  STAMP(0); CSTAMP(14);
  const __amdgpu_buffer_rsrc_t srdK = __builtin_amdgcn_make_buffer_rsrc((void*)(kc + (size_t)(b * NH + h) * SK * DH), (short)0, SK * DH * 2, 0x00020000);
  const __amdgpu_buffer_rsrc_t srdV = __builtin_amdgcn_make_buffer_rsrc((void*)(vc + (size_t)(b * NH + h) * SK * DH), (short)0, SK * DH * 2, 0x00020000);
  const unsigned w4 = w & 3u;
  const unsigned prow = 4u * w4 + (lane >> 4);
  const unsigned koff = prow * 256u + (((lane & 15u) ^ (prow & 15u)) << 4);
  const unsigned voff = prow * 256u + (((lane & 15u) ^ (((prow & 3u) << 2) | (((prow >> 2) & 1u) << 1))) << 4);
  auto dma_k = [&](unsigned t) {
    const unsigned m = ldsb + KR + (t & 7u) * 8192u + w4 * 1024u;
    dma_nt(m, koff, srdK, t * 8192u); dma_nt(m + 4096u, koff, srdK, t * 8192u + 4096u);
  };
  auto dma_v = [&](unsigned t) {
    const unsigned m = ldsb + VR + (t & 7u) * 8192u + w4 * 1024u;
    dma_nt(m, voff, srdV, t * 8192u); dma_nt(m + 4096u, voff, srdV, t * 8192u + 4096u);
  };
  const unsigned q4 = (lane & 15u) >> 2, p = lane & 3u;

  if (w < 4) {
    const unsigned r32 = lane & 31u, hh = lane >> 5, blk = (lane >> 4) & 1u;
    dma_k(0); dma_k(1); dma_v(0); dma_k(2); dma_v(1); dma_k(3); dma_v(2);
    MainState st;
    {
      f32x16 acc[4];
#pragma unroll
      for (int dt = 0; dt < 4; ++dt)
#pragma unroll
        for (int r = 0; r < 16; ++r) acc[dt][r] = 0.f;
      unsigned xa[4], wa[4];
      const unsigned xrow = 32u * w + r32;
#pragma unroll
      for (int s = 0; s < 4; ++s) xa[s] = ldsb + XS0 + xrow * 128u + (((2u * s + hh) ^ ((xrow >> 1) & 7u)) << 4);
#pragma unroll
      for (int dt = 0; dt < 4; ++dt) wa[dt] = ldsb + (8u * hh + q4) * 256u + ((((dt ^ q4) << 2) | ((2u * blk + (p & 1u)) ^ (hh << 1))) << 4) + 8u * (p >> 1);
#pragma unroll
      for (int sg = 0; sg < 10; ++sg) {
        const unsigned wsl = (sg % 3 == 0) ? WS0 : (sg % 3 == 1) ? WS1 : WS2, xsl = (sg % 3) * XSZ;
        bar();
#pragma unroll
        for (int s = 0; s < 4; ++s) {
          const bf16x8 xf = lds128(xa[s] + xsl);
#pragma unroll
          for (int dt = 0; dt < 4; ++dt) {
            const unsigned a = wa[dt] + wsl + s * 4096;
            acc[dt] = MFMA32(cat(ldstr(a), ldstr(a + 1024)), xf, acc[dt]);
          }
        }
      }
#pragma unroll
      for (int dt = 0; dt < 4; ++dt)
#pragma unroll
        for (int s = 0; s < 2; ++s) {
          u32x4 q;
#pragma unroll
          for (int e = 0; e < 4; ++e) q[e] = cvtpk(acc[dt][8 * s + 2 * e] * QSCALE, acc[dt][8 * s + 2 * e + 1] * QSCALE);
          st.Qf[2 * dt + s] = __builtin_bit_cast(bf16x8, q);
        }
    }
    STAMP(1);
    waitvm<0>();
    bar();
    STAMP(2);
#pragma unroll
    for (int s = 0; s < 8; ++s) st.ka[s] = ldsb + KR + r32 * 256u + (((2u * s + hh) ^ (r32 & 15u)) << 4);
#pragma unroll
    for (int dt = 0; dt < 4; ++dt) st.va[dt] = ldsb + VR + (4u * hh + q4) * 256u + ((((dt ^ q4) << 2) | ((2u * blk + (p & 1u)) ^ (hh << 1))) << 4) + 8u * (p >> 1);
    f32x16 SA, SB;
    {
#pragma unroll
      for (int s = 0; s < 8; ++s) st.Kf[s] = lds128(st.ka[s]);
#pragma unroll
      for (int r = 0; r < 16; ++r) SA[r] = 0.f;
#pragma unroll
      for (int s = 0; s < 8; ++s) SA = MFMA32(st.Kf[s], st.Qf[s], SA);
#pragma unroll
      for (int s = 0; s < 8; ++s) st.Kf[s] = lds128(st.ka[s] + 8192);
      float m = SA[0];
#pragma unroll
      for (int r = 1; r < 16; ++r) m = fmaxf(m, SA[r]);
      m = swap32_max(m);
#pragma unroll
      for (int r = 0; r < 16; ++r) { SA[r] -= m; st.NEGM[r] = -m; }
#pragma unroll
      for (int dt = 0; dt < 4; ++dt)
#pragma unroll
        for (int r = 0; r < 16; ++r) st.O[dt][r] = 0.f;
      st.l = 0.f;
    }
    for (int rnd = 0; rnd < 4; ++rnd) {
      main_iter<0>(st, SA, SB, false);
      main_iter<1>(st, SB, SA, false);
      main_iter<2>(st, SA, SB, false);
      main_iter<3>(st, SB, SA, false);
      main_iter<4>(st, SA, SB, false);
      main_iter<5>(st, SB, SA, false);
      main_iter<6>(st, SA, SB, false);
      main_iter<7>(st, SB, SA, rnd == 3);
      if (rnd == 0) STAMP(3); else if (rnd == 1) STAMP(4); else if (rnd == 2) STAMP(5); else STAMP(6);
    }
    bar();
    const float inv = 1.0f / swap32_add(st.l);
    const unsigned q = 32u * w + r32;
    char* ob = (char*)ot + ((size_t)(b * 9u + (q >> 4)) * 128u + h * 4u) * 1024u + (hh * 16u + (q & 15u)) * 16u;
#pragma unroll
    for (int dt = 0; dt < 4; ++dt)
#pragma unroll
      for (int s = 0; s < 2; ++s) {
        u32x4 o;
#pragma unroll
        for (int e = 0; e < 4; ++e) o[e] = cvtpk(st.O[dt][8 * s + 2 * e] * inv, st.O[dt][8 * s + 2 * e + 1] * inv);
        *(u32x4*)(ob + dt * 1024 + s * 512) = o;
      }
    waitvm<0>(); STAMP(7); CSTAMP(15); STAMP_OUT(0, 8); STAMP_OUT(14, 16);
  } else {
    const unsigned r16 = lane & 15u, g4 = lane >> 4;
    const __amdgpu_buffer_rsrc_t srdX = __builtin_amdgcn_make_buffer_rsrc((void*)(x + (size_t)b * SQ * DM), (short)0, SQ * DM * 2, 0x00020000);
    const __amdgpu_buffer_rsrc_t srdW = __builtin_amdgcn_make_buffer_rsrc((void*)(wq + h * DH), (short)0, (DM * HD - h * DH) * 2, 0x00020000);
    const unsigned xoff = (8u * w4 + (lane >> 3)) * (DM * 2u) + (((lane & 7u) ^ (4u * (w4 & 1u) + (lane >> 4))) << 4);
    const unsigned woff = prow * (HD * 2u) + (((lane & 15u) ^ (((lane >> 4) << 2) | (((w4 >> 1) & 1u) << 1))) << 4);
    const unsigned rot = (wi * 3u) % 10u;
    auto dma_stage = [&](int sg) {
      unsigned kk = sg + rot; kk = kk >= 10u ? kk - 10u : kk;
      const unsigned wsl = (sg % 3 == 0) ? WS0 : (sg % 3 == 1) ? WS1 : WS2, xsl = XS0 + (sg % 3) * XSZ;
#pragma unroll
      for (int i = 0; i < 4; ++i) {
        dma(ldsb + wsl + (w4 + 4u * i) * 1024u, woff, srdW, (kk * 64u + 16u * i) * (HD * 2u));
        dma(ldsb + xsl + (w4 + 4u * i) * 1024u, xoff, srdX, kk * 128u + 32u * i * (DM * 2u));
      }
      if (w4 < 2) dma(ldsb + xsl + (w4 + 16u) * 1024u, xoff, srdX, kk * 128u + 128u * (DM * 2u));
    };
    dma_stage(0); dma_stage(1); dma_stage(2);
    bf16x8 Qf[4];
    {
      f32x4 acc[2] = {{0.f, 0.f, 0.f, 0.f}, {0.f, 0.f, 0.f, 0.f}};
      unsigned xa[2], wa[2];
      const unsigned xrow = 128u + r16;
#pragma unroll
      for (int s = 0; s < 2; ++s) xa[s] = ldsb + XS0 + xrow * 128u + (((4u * s + g4) ^ ((xrow >> 1) & 7u)) << 4);
#pragma unroll
      for (int e = 0; e < 2; ++e) wa[e] = ldsb + (8u * g4 + q4) * 256u + (((4u * w4 + 2u * e + (p >> 1)) ^ ((q4 << 2) | ((g4 & 1u) << 1))) << 4) + 8u * (p & 1u);
#pragma unroll
      for (int sg = 0; sg < 10; ++sg) {
        const unsigned wsl = (sg % 3 == 0) ? WS0 : (sg % 3 == 1) ? WS1 : WS2, xsl = (sg % 3) * XSZ;
        if (sg == 0) { if (w4 < 2) waitvm<18>(); else waitvm<16>(); }
        else if (sg < 9) { if (w4 < 2) waitvm<9>(); else waitvm<8>(); }
        else waitvm<0>();
        bar();
        if (sg >= 1 && sg + 2 < 10) dma_stage(sg + 2);
#pragma unroll
        for (int s = 0; s < 2; ++s) {
          const bf16x8 xf = lds128(xa[s] + xsl);
#pragma unroll
          for (int e = 0; e < 2; ++e) {
            const unsigned a = wa[e] + wsl + s * 8192;
            acc[e] = MFMA16(cat(ldstr(a), ldstr(a + 1024)), xf, acc[e]);
          }
        }
      }
#pragma unroll
      for (int e = 0; e < 2; ++e) {
        u32x2 q = {cvtpk(acc[e][0] * QSCALE, acc[e][1] * QSCALE), cvtpk(acc[e][2] * QSCALE, acc[e][3] * QSCALE)};
        *(LAS u32x2*)(ldsb + QX + w4 * 1024u + ((2u * e + (g4 >> 1)) * 16u + r16) * 16u + (g4 & 1u) * 8u) = q;
      }
    }
    STAMP(1);
    bar();
    STAMP(2);
    dma_k(4); dma_v(3); dma_k(5); dma_v(4); dma_k(6); dma_v(5); dma_k(7);
#pragma unroll
    for (int s = 0; s < 4; ++s) Qf[s] = lds128(ldsb + QX + s * 1024u + lane * 16u);
    unsigned ka[4], va[4];
#pragma unroll
    for (int s = 0; s < 4; ++s) ka[s] = ldsb + KR + r16 * 256u + (((4u * s + g4) ^ r16) << 4);
#pragma unroll
    for (int u = 0; u < 4; ++u) va[u] = ldsb + VR + (4u * g4 + q4) * 256u + (((4u * u + (p >> 1)) ^ ((q4 << 2) | ((g4 & 1u) << 1))) << 4) + 8u * (p & 1u);
    f32x4 O[8];
#pragma unroll
    for (int i = 0; i < 8; ++i) O[i] = f32x4{0.f, 0.f, 0.f, 0.f};
    float m = -__builtin_inff(), l = 0.f;
    bf16x8 Pk;
    auto qk_rem = [&](unsigned t) {
      const unsigned so = (t & 7u) * 8192u;
      f32x4 S[2] = {{0.f, 0.f, 0.f, 0.f}, {0.f, 0.f, 0.f, 0.f}};
#pragma unroll
      for (int s = 0; s < 4; ++s)
#pragma unroll
        for (int kt = 0; kt < 2; ++kt) S[kt] = MFMA16(lds128(ka[s] + so + kt * 4096), Qf[s], S[kt]);
      float mx = fmaxf(fmaxf(fmaxf(S[0][0], S[0][1]), fmaxf(S[0][2], S[0][3])), fmaxf(fmaxf(S[1][0], S[1][1]), fmaxf(S[1][2], S[1][3])));
      mx = swap32_max(swap16_max(mx));
      const float mn = fmaxf(m, mx), f = __builtin_amdgcn_exp2f(m - mn);
      m = mn;
      float sum = 0.f;
#pragma unroll
      for (int kt = 0; kt < 2; ++kt)
#pragma unroll
        for (int r = 0; r < 4; ++r) { S[kt][r] = __builtin_amdgcn_exp2f(S[kt][r] - mn); sum += S[kt][r]; }
      l = l * f + sum;
#pragma unroll
      for (int i = 0; i < 8; ++i) O[i] *= f;
      u32x4 pk = {cvtpk(S[0][0], S[0][1]), cvtpk(S[0][2], S[0][3]), cvtpk(S[1][0], S[1][1]), cvtpk(S[1][2], S[1][3])};
      Pk = __builtin_bit_cast(bf16x8, pk);
    };
    auto pv_rem = [&](unsigned t) {
      const unsigned so = (t & 7u) * 8192u;
#pragma unroll
      for (int u = 0; u < 4; ++u)
#pragma unroll
        for (int e = 0; e < 2; ++e) {
          const unsigned a = (va[u] ^ (e * 32u)) + so;
          O[2 * u + e] = MFMA16(cat(ldstr(a), ldstr(a + 4096)), Pk, O[2 * u + e]);
        }
    };
    if (w4 < 2) qk_rem(w4);
    for (unsigned t = 0; t < 32; ++t) {
      waitvm<24>();
      bar();
      if (t == 0) { dma_v(6); dma_k(8); }
      dma_v(t + 7); dma_k(t + 9);
      if ((t & 3u) == w4) pv_rem(t);
      if (((t + 2) & 3u) == w4 && t < 30) qk_rem(t + 2);
      if (t == 7) STAMP(3); else if (t == 15) STAMP(4); else if (t == 23) STAMP(5); else if (t == 31) STAMP(6);
    }
    l = swap32_add(swap16_add(l));
    if (g4 == 0) *(LAS f32x2*)(ldsb + OX + 24576u + (w4 * 16u + r16) * 8u) = f32x2{m, l};
#pragma unroll
    for (int u = 0; u < 4; ++u)
      if (u != (int)w4) {
        const unsigned slot = (w4 - 1u - u) & 3u;
#pragma unroll
        for (int e = 0; e < 2; ++e) *(LAS f32x4*)(ldsb + OX + ((u * 3u + slot) * 2u + e) * 1024u + lane * 16u) = O[2 * u + e];
      }
    waitvm<0>();
    bar();
    f32x2 ml[4];
#pragma unroll
    for (int j = 0; j < 4; ++j) ml[j] = *(LAS f32x2*)(ldsb + OX + 24576u + (j * 16u + r16) * 8u);
    const float M = fmaxf(fmaxf(ml[0][0], ml[1][0]), fmaxf(ml[2][0], ml[3][0]));
    float L = 0.f;
#pragma unroll
    for (int j = 0; j < 4; ++j) L += ml[j][1] * __builtin_amdgcn_exp2f(ml[j][0] - M);
    const float inv = 1.0f / L;
    f32x4 o[2];
    {
      const float f = __builtin_amdgcn_exp2f(m - M) * inv;
#pragma unroll
      for (int e = 0; e < 2; ++e) {
        f32x4 own = O[e];
        if (w4 == 1) own = O[2 + e]; else if (w4 == 2) own = O[4 + e]; else if (w4 == 3) own = O[6 + e];
        o[e] = own * f;
      }
    }
#pragma unroll
    for (int slot = 0; slot < 3; ++slot) {
      const unsigned src = (w4 + 1u + slot) & 3u;
      const float msrc = (*(LAS f32x2*)(ldsb + OX + 24576u + (src * 16u + r16) * 8u))[0];
      const float f = __builtin_amdgcn_exp2f(msrc - M) * inv;
#pragma unroll
      for (int e = 0; e < 2; ++e) o[e] += *(LAS f32x4*)(ldsb + OX + ((w4 * 3u + slot) * 2u + e) * 1024u + lane * 16u) * f;
    }
    char* ob = (char*)ot + ((size_t)(b * 9u + 8u) * 128u + h * 4u + w4) * 1024u;
#pragma unroll
    for (int e = 0; e < 2; ++e) {
      u32x2 q = {cvtpk(o[e][0], o[e][1]), cvtpk(o[e][2], o[e][3])};
      *(u32x2*)(ob + ((2u * e + (g4 >> 1)) * 16u + r16) * 16u + (g4 & 1u) * 8u) = q;
    }
    waitvm<0>(); STAMP(7); CSTAMP(15); STAMP_OUT(0, 8); STAMP_OUT(14, 16);
  }
}

constexpr unsigned LDS_B = 8 * 14336;
__global__ __launch_bounds__(512, 2) void oproj_kernel(const u16* __restrict__ ot, const u16* __restrict__ wo, u16* __restrict__ out, unsigned long long* __restrict__ stamps) {
  extern __shared__ __attribute__((aligned(16))) char lds[];
  const unsigned ldsb = (unsigned)(size_t)(LAS char*)lds;
  const unsigned lane = threadIdx.x & 63u;
  const unsigned w = rfl(threadIdx.x >> 6);
  const unsigned bid = blockIdx.x, xg = bid & 7u, wi = bid >> 3;
  const unsigned mi = (xg >> 1) * 6u + wi / 5u, ni = (xg & 1u) * 5u + wi % 5u;
  STAMP_DECL;
  STAMP(8);
  const __amdgpu_buffer_rsrc_t srdA = __builtin_amdgcn_make_buffer_rsrc((void*)((const char*)ot + (size_t)mi * 3u * 128u * 1024u), (short)0, 3 * 128 * 1024, 0x00020000);
  const __amdgpu_buffer_rsrc_t srdB = __builtin_amdgcn_make_buffer_rsrc((void*)(wo + ni * 64u), (short)0, (HD * DM - ni * 64u) * 2, 0x00020000);
  const unsigned ring = ldsb + w * 14336u;
  const unsigned aoff = lane * 16u;
  unsigned boff[2];
#pragma unroll
  for (int par = 0; par < 2; ++par) boff[par] = (lane >> 3) * (DM * 2u) + (((lane & 7u) ^ ((((lane >> 4) & 1u) | (par << 1)) << 1)) << 4);
  auto issue = [&](unsigned j) {
    const unsigned ks = w * 16u + j, base = ring + (j & 1u) * 7168u;
#pragma unroll
    for (int i = 0; i < 4; ++i) dma(base + i * 1024u, boff[i & 1], srdB, (ks * 32u + 8u * i) * (DM * 2u));
#pragma unroll
    for (int i = 0; i < 3; ++i) dma(base + 4096u + i * 1024u, aoff, srdA, (i * 128u + ks) * 1024u);
  };
  issue(0); issue(1);
  const unsigned g4 = lane >> 4, q4 = (lane & 15u) >> 2, p = lane & 3u;
  unsigned ba[4];
#pragma unroll
  for (int nt = 0; nt < 4; ++nt) { const unsigned f = ((q4 >> 1) & 1u) | ((g4 & 1u) << 1); ba[nt] = ring + (8u * g4 + q4) * 128u + ((((unsigned)nt ^ f) * 2u + (p >> 1)) << 4) + 8u * (p & 1u); }
  f32x4 acc[4][3];
#pragma unroll
  for (int nt = 0; nt < 4; ++nt)
#pragma unroll
    for (int mt = 0; mt < 3; ++mt) acc[nt][mt] = f32x4{0.f, 0.f, 0.f, 0.f};
#pragma unroll
  for (int j = 0; j < 16; ++j) {
    if (j < 15) waitvm<7>(); else waitvm<0>();
    bf16x8 bf[4], af[3];
#pragma unroll
    for (int nt = 0; nt < 4; ++nt) { const unsigned a = ba[nt] + (j & 1) * 7168; bf[nt] = cat(ldstr(a), ldstr(a + 512)); }
#pragma unroll
    for (int mt = 0; mt < 3; ++mt) af[mt] = lds128(ring + (j & 1) * 7168 + 4096 + mt * 1024 + lane * 16u);
    asm volatile("s_waitcnt lgkmcnt(0)" : "+v"(bf[0]), "+v"(bf[1]), "+v"(bf[2]), "+v"(bf[3]), "+v"(af[0]), "+v"(af[1]), "+v"(af[2])::"memory");
    if (j + 2 < 16) issue(j + 2);
#pragma unroll
    for (int nt = 0; nt < 4; ++nt)
#pragma unroll
      for (int mt = 0; mt < 3; ++mt) acc[nt][mt] = MFMA16(bf[nt], af[mt], acc[nt][mt]);
  }
  const unsigned r16 = lane & 15u;
#pragma unroll
  for (int nt = 0; nt < 4; ++nt)
#pragma unroll
    for (int mt = 0; mt < 3; ++mt) *(LAS f32x4*)(ring + (mt * 16u + r16) * 256u + (((nt * 4u + g4) ^ r16) << 4)) = acc[nt][mt];
  bar();
  if (threadIdx.x < 384) {
    const unsigned m = threadIdx.x >> 3, n8 = threadIdx.x & 7u;
    f32x4 s0 = {0.f, 0.f, 0.f, 0.f}, s1 = s0;
#pragma unroll
    for (int j = 0; j < 8; ++j) {
      s0 += *(LAS f32x4*)(ldsb + j * 14336u + m * 256u + (((2u * n8) ^ (m & 15u)) << 4));
      s1 += *(LAS f32x4*)(ldsb + j * 14336u + m * 256u + (((2u * n8 + 1u) ^ (m & 15u)) << 4));
    }
    u32x4 o = {cvtpk(s0[0], s0[1]), cvtpk(s0[2], s0[3]), cvtpk(s1[0], s1[1]), cvtpk(s1[2], s1[3])};
    *(u32x4*)(out + (size_t)(mi * 48u + m) * DM + ni * 64u + n8 * 8u) = o;
  }
  waitvm<0>(); STAMP(9); STAMP_OUT(8, 10);
}

extern "C" void kernel_launch(void* const* d_in, const int* in_sizes, int n_in, void* d_out, int out_size, void* d_ws, size_t ws_size, hipStream_t stream) {
  const u16* x = (const u16*)d_in[0]; const u16* k = (const u16*)d_in[1]; const u16* v = (const u16*)d_in[2];
  const u16* wq = (const u16*)d_in[3]; const u16* wo = (const u16*)d_in[4];
  u16* ot = (u16*)d_ws;
  unsigned long long* stamps = (unsigned long long*)((char*)d_ws + (16u << 20));
  (void)hipFuncSetAttribute((const void*)attn_kernel, hipFuncAttributeMaxDynamicSharedMemorySize, LDS_A);
  (void)hipFuncSetAttribute((const void*)oproj_kernel, hipFuncAttributeMaxDynamicSharedMemorySize, LDS_B);
  attn_kernel<<<256, 512, LDS_A, stream>>>(x, k, v, wq, ot, stamps);
  oproj_kernel<<<240, 512, LDS_B, stream>>>(ot, wo, (u16*)d_out, stamps);
}
```

```cpp
#include <hip/hip_runtime.h>
#include <hip/hip_bf16.h>
#include <utility>

typedef unsigned short u16;
typedef float f32x16 __attribute__((ext_vector_type(16)));
typedef float f32x4 __attribute__((ext_vector_type(4)));
typedef float f32x2 __attribute__((ext_vector_type(2)));
typedef __bf16 bf16x8 __attribute__((ext_vector_type(8)));
typedef __bf16 bf16x4 __attribute__((ext_vector_type(4)));
typedef __bf16 bf16x2 __attribute__((ext_vector_type(2)));
typedef unsigned u32x4 __attribute__((ext_vector_type(4)));
typedef unsigned u32x2 __attribute__((ext_vector_type(2)));
#define LAS __attribute__((address_space(3)))
#define DI __device__ __forceinline__

constexpr int NB = 8, SQ = 144, DM = 640, NH = 32, SK = 1024, DH = 128;
constexpr int HD = NH * DH;
constexpr float QSCALE = 0.12751743082f;
constexpr float THR = 8.0f;

#define STAMPS 0

constexpr unsigned KR = 0, VR = 65536, SLOT = 16384;
constexpr unsigned WBASE = 32768;
constexpr unsigned XS0 = 98304, XSZ = 18432;
constexpr unsigned QI = XS0 + XSZ;
constexpr unsigned QREM = 159744;
constexpr unsigned STATS = 155648;
constexpr unsigned PX = 49152, RX = 131072;
constexpr unsigned BSZ = 24576;
constexpr size_t WS_MBOX = 9437184, MBOX = 46080;
constexpr size_t WS_FLAG = WS_MBOX + 256 * MBOX;
typedef __attribute__((address_space(1))) unsigned long long gu64;
constexpr unsigned LDS_A = 163840;
constexpr unsigned wslot(int sg) { return sg % 3 == 0 ? 16384u : sg % 3 == 1 ? 0u : 49152u; }

DI unsigned rfl(unsigned x) { return __builtin_amdgcn_readfirstlane(x); }
DI unsigned cvtpk(float lo, float hi) { f32x2 v = {lo, hi}; bf16x2 b = __builtin_convertvector(v, bf16x2); return __builtin_bit_cast(unsigned, b); }
DI bf16x8 lds128(unsigned a) { return *(LAS bf16x8*)a; }
DI bf16x4 ldstr(unsigned a) { return __builtin_amdgcn_ds_read_tr16_b64_v4bf16((LAS bf16x4*)a); }
DI bf16x8 cat(bf16x4 a, bf16x4 b) { return __builtin_shufflevector(a, b, 0, 1, 2, 3, 4, 5, 6, 7); }
#define MFMA32(a, b, c) __builtin_amdgcn_mfma_f32_32x32x16_bf16((a), (b), (c), 0, 0, 0)
#define MFMA16(a, b, c) __builtin_amdgcn_mfma_f32_16x16x32_bf16((a), (b), (c), 0, 0, 0)
template <int N> DI void waitvm() { asm volatile("s_waitcnt vmcnt(%0)" ::"i"(N) : "memory"); }
DI void bar() { asm volatile("s_waitcnt lgkmcnt(0)\n\ts_barrier" ::: "memory"); }
DI void dma(unsigned m0, unsigned voff, __amdgpu_buffer_rsrc_t srd, unsigned soff) {
  asm volatile("s_mov_b32 m0, %0\n\ts_nop 0\n\tbuffer_load_dwordx4 %1, %2, %3 offen lds" ::"s"(m0), "v"(voff), "s"(srd), "s"(soff) : "memory");
}
DI void dma_nt(unsigned m0, unsigned voff, __amdgpu_buffer_rsrc_t srd, unsigned soff) {
  asm volatile("s_mov_b32 m0, %0\n\ts_nop 0\n\tbuffer_load_dwordx4 %1, %2, %3 offen nt lds" ::"s"(m0), "v"(voff), "s"(srd), "s"(soff) : "memory");
}
DI float swap32_max(float x) { auto r = __builtin_amdgcn_permlane32_swap(__float_as_uint(x), __float_as_uint(x), false, false); return fmaxf(__uint_as_float(r[0]), __uint_as_float(r[1])); }
DI float swap32_add(float x) { auto r = __builtin_amdgcn_permlane32_swap(__float_as_uint(x), __float_as_uint(x), false, false); return __uint_as_float(r[0]) + __uint_as_float(r[1]); }
DI float swap16_max(float x) { auto r = __builtin_amdgcn_permlane16_swap(__float_as_uint(x), __float_as_uint(x), false, false); return fmaxf(__uint_as_float(r[0]), __uint_as_float(r[1])); }
DI float swap16_add(float x) { auto r = __builtin_amdgcn_permlane16_swap(__float_as_uint(x), __float_as_uint(x), false, false); return __uint_as_float(r[0]) + __uint_as_float(r[1]); }
DI float max3f(float a, float b, float c) { return __builtin_fmaxf(__builtin_fmaxf(a, b), c); }

#define STAMP_DECL
#define STAMP_ARG
#define STAMP_PASS
#define STAMP_END
#define STAMP(i) do {} while (0)
#define STAMP_OUT(lo, hi) do {} while (0)

DI float fadd(float a, float b) { float r; asm("v_add_f32_e32 %0, %1, %2" : "=v"(r) : "v"(a), "v"(b)); return r; }
#define GAP() __builtin_amdgcn_sched_barrier(0)
const f32x4 kZero4 = {0.f, 0.f, 0.f, 0.f};

#define TOFF(x) (x)
#define FSTAMP(i) do {} while (0)
#define DMAXW dma
#define DMAB dma
#define ST_AUX 16
#define LD_AUX 16
struct Att {
  f32x4 O[2][8], OR[8];
  bf16x8 Q[2][4];
  bf16x8 F[8];
  f32x4 S[2][2], SR[2];
  f32x4 T[2];
  u32x4 P0, P0n, P1, PR;
  float l[2], lr;
  unsigned ka[4], va[8], qra;
  unsigned koff, voff, m0k, m0v, rot;
  __amdgpu_buffer_rsrc_t srdK, srdV, srdB;
  unsigned boff[3], ldsb, j, w4;
};
DI unsigned fresh_lane() { unsigned l; asm volatile("v_mbcnt_lo_u32_b32 %0, -1, 0\n\tv_mbcnt_hi_u32_b32 %0, -1, %0" : "=v"(l)); return l; }
DI void set_boff(Att& a, unsigned lane) {
#pragma unroll
  for (int i = 0; i < 3; ++i) { const unsigned qd = 16u * i + (lane >> 2), row = qd / 3u; a.boff[i] = row * (DM * 2u) + (qd - 3u * row) * 64u + (((lane & 3u) ^ (((row >> 2) & 1u) << 1)) << 4); }
}
DI void dma_b(const Att& a, unsigned s, unsigned rb) {
  const unsigned m = a.ldsb + (s >= 6u ? s - 6u : s) * BSZ + rb * 3072u, so = ((((a.j + s) & 7u) << 7) + rb * 16u) * (DM * 2u);
#pragma unroll
  for (int i = 0; i < 3; ++i) DMAB(m + i * 1024u, a.boff[i], a.srdB, so);
}
DI void mma(f32x4& d, bf16x8 a, bf16x8 b) { asm volatile("v_mfma_f32_16x16x32_bf16 %0, %1, %2, %0" : "+v"(d) : "v"(a), "v"(b)); GAP(); }
DI void mma0(f32x4& d, bf16x8 a, bf16x8 b) { asm volatile("v_mfma_f32_16x16x32_bf16 %0, %1, %2, 0" : "=&v"(d) : "v"(a), "v"(b)); GAP(); }
DI void mmap(f32x4& d, bf16x8 a, u32x4 b) { asm volatile("v_mfma_f32_16x16x32_bf16 %0, %1, %2, %0" : "+v"(d) : "v"(a), "v"(b)); GAP(); }
template <int E> DI void sm_exp(f32x4 (&s)[2]) { s[E >> 2][E & 3] = __builtin_amdgcn_exp2f(s[E >> 2][E & 3]); GAP(); }
template <int E> DI void sm_fin(f32x4 (&s)[2], float& l, u32x4& p) {
  if constexpr (E & 1) { p[E >> 1] = cvtpk(s[E >> 2][(E & 3) - 1], s[E >> 2][E & 3]); GAP(); }
  l = fadd(l, s[E >> 2][E & 3]); GAP();
}
template <int SL, int ROLE, bool FIN, int M> DI void gapA(Att& a, bf16x8 (&qr)[4], unsigned so STAMP_ARG) {
  constexpr int kt = M >> 3, s = (M >> 1) & 3, qt = M & 1;
  constexpr bool LAST = FIN && SL == 3;
  if constexpr (!LAST) {
    if constexpr (s == 0) mma0(a.S[qt][kt], a.F[kt * 4 + s], a.Q[qt][s]); else mma(a.S[qt][kt], a.F[kt * 4 + s], a.Q[qt][s]);
  }
  if constexpr (M == 3) {
    if constexpr (!FIN && SL == 1) FSTAMP(12);
    if constexpr (ROLE == 0) waitvm<(!FIN || SL == 0) ? 8 : SL == 1 ? 4 : SL == 2 ? 10 : 12>();
    if constexpr (!FIN && SL == 1) FSTAMP(10);
    bar(); GAP();
    if constexpr (!FIN && SL == 1) FSTAMP(11);
  }
  if constexpr (!(M & 1)) sm_exp<(M >> 1)>(a.T); else { if constexpr (LAST) asm volatile("s_nop 0"); sm_fin<(M >> 1)>(a.T, a.l[1], a.P1); }
  if constexpr (!LAST && ROLE == 1 && qt == 1) {
    if constexpr (s == 0) mma0(a.SR[kt], a.F[kt * 4 + s], qr[s]); else mma(a.SR[kt], a.F[kt * 4 + s], qr[s]);
  }
  if constexpr (M >= 3 && (M & 1)) {
    if constexpr (M == 3) { a.F[0] = cat(ldstr(a.va[0] + SL * SLOT), ldstr(a.va[0] + SL * SLOT + 4096)); GAP(); }
    constexpr int j = M >> 1;
    a.F[j] = cat(ldstr(a.va[j] + SL * SLOT), ldstr(a.va[j] + SL * SLOT + 4096)); GAP();
  }
  if constexpr (ROLE == 2 && (M & 3) == 1) sm_exp<4 + (M >> 2)>(a.SR);
  if constexpr (ROLE == 2 && (M & 3) == 2) sm_fin<4 + (M >> 2)>(a.SR, a.lr, a.PR);
  if constexpr (ROLE == 0 && (!FIN || SL <= 1) && M >= 6 && M <= 12 && !(M & 1)) {
    constexpr int j = (M - 6) >> 1;
    dma_nt(a.m0v + ((SL + 2) & 3) * SLOT + j * 4096u, a.voff, a.srdV, TOFF(so + (SL + 2u) * SLOT) + j * 4096u); GAP();
  }
}
template <int SL, int ROLE, bool FIN, int M> DI void gapB(Att& a, bf16x8 (&qr)[4], unsigned so) {
  constexpr int dt = M >> 1, qt = M & 1;
  constexpr bool LAST = FIN && SL == 3;
  mmap(a.O[qt][dt], a.F[dt], qt ? a.P1 : a.P0);
  if constexpr (!LAST) { if constexpr (!(M & 1)) sm_exp<(M >> 1)>(a.S[0]); else sm_fin<(M >> 1)>(a.S[0], a.l[0], a.P0n); }
  if constexpr (ROLE == 2 && qt == 1) mmap(a.OR[dt], a.F[dt], a.PR);
  if constexpr (!LAST) {
    if constexpr ((M & 1) && !(FIN && SL == 2)) { a.F[dt] = lds128(a.ka[dt & 3] + ((SL + 2) & 3) * SLOT + (dt >> 2) * 4096); GAP(); }
    if constexpr (ROLE == 2 && M >= 8 && !(M & 1) && !(FIN && SL == 2)) { qr[(M - 8) >> 1] = lds128(a.qra + ((M - 8) >> 1) * 1024); GAP(); }
    if constexpr (ROLE == 1 && (M & 3) == 1) sm_exp<(M >> 2)>(a.SR);
    if constexpr (ROLE == 1 && (M & 3) == 2) sm_fin<(M >> 2)>(a.SR, a.lr, a.PR);
    if constexpr (ROLE == 0 && FIN && SL >= 1 && (M & 7) == 2) { dma_b(a, SL - 1, 2u * a.w4 + (M >> 3)); GAP(); }
    if constexpr (ROLE == 0 && !FIN && (M & 3) == 2) {
      constexpr int j = M >> 2;
      dma_nt(a.m0k + SL * SLOT + j * 4096u, a.koff, a.srdK, TOFF(so + (SL + 4u) * SLOT) + j * 4096u); GAP();
    }
  }
}
template <int SL, int ROLE, bool FIN, int... Ms> DI void step_(Att& a, bf16x8 (&qr)[4], unsigned so, std::integer_sequence<int, Ms...> STAMP_ARG) {
  (gapA<SL, ROLE, FIN, Ms>(a, qr, so STAMP_PASS), ...);
  if constexpr (!FIN && SL == 1) FSTAMP(14);
  if constexpr (FIN && SL == 3) asm volatile("s_nop 1");
  (gapB<SL, ROLE, FIN, Ms>(a, qr, so), ...);
  if constexpr (!FIN && SL == 1) FSTAMP(15);
  a.T[0] = a.S[1][0]; a.T[1] = a.S[1][1]; a.P0 = a.P0n;
}
template <int SL, int ROLE, bool FIN> DI void step(Att& a, bf16x8 (&qr)[4], unsigned so STAMP_ARG) { step_<SL, ROLE, FIN>(a, qr, so, std::make_integer_sequence<int, 16>{} STAMP_PASS); }

template <int KIND> DI void attention(Att& a, unsigned ldsb, unsigned lane, unsigned w, unsigned b, unsigned h, char* ws, u16* __restrict__ out, unsigned long long tag STAMP_ARG) {
  constexpr int RE = KIND == 0 ? 0 : KIND == 1 ? 2 : 1, RO = KIND == 0 ? 0 : KIND == 1 ? 1 : 2;
  bf16x8 qr[4];
  const unsigned qg = w & 3u;
#pragma unroll
  for (int j = 0; j < 8; ++j) a.F[j] = lds128(a.ka[j & 3] + (j >> 2) * 4096);
#pragma unroll
  for (int qt = 0; qt < 2; ++qt)
#pragma unroll
    for (int s = 0; s < 4; ++s) a.Q[qt][s] = lds128(ldsb + QI + (2u * qg + (qt ^ (w >> 2))) * 4096u + s * 1024u + lane * 16u);
  if constexpr (KIND != 0) {
#pragma unroll
    for (int s = 0; s < 4; ++s) qr[s] = lds128(a.qra + s * 1024);
  }
  if constexpr (KIND == 0) waitvm<4>();
  bar();
  if constexpr (KIND == 0) {
    _Pragma("unroll") for (int j = 0; j < 4; ++j) dma_nt(a.m0k + 2u * SLOT + j * 4096u, a.koff, a.srdK, TOFF(2u * SLOT) + j * 4096u);
  }
  GAP();
#pragma unroll
  for (int kt = 0; kt < 2; ++kt)
#pragma unroll
    for (int s = 0; s < 4; ++s) {
      if (s == 0) mma0(a.S[0][kt], a.F[kt * 4 + s], a.Q[0][s]); else mma(a.S[0][kt], a.F[kt * 4 + s], a.Q[0][s]);
      if (s == 0) mma0(a.T[kt], a.F[kt * 4 + s], a.Q[1][s]); else mma(a.T[kt], a.F[kt * 4 + s], a.Q[1][s]);
      if constexpr (KIND == 1) { if (s == 0) mma0(a.SR[kt], a.F[kt * 4 + s], qr[s]); else mma(a.SR[kt], a.F[kt * 4 + s], qr[s]); }
      a.F[kt * 4 + s] = lds128(a.ka[s] + SLOT + kt * 4096); GAP();
    }
#pragma unroll
  for (int dt = 0; dt < 8; ++dt) { a.O[0][dt] = kZero4; a.O[1][dt] = kZero4; a.OR[dt] = kZero4; }
  a.l[0] = a.l[1] = a.lr = 0.f;
  asm volatile("s_nop 7"); GAP();
  sm_exp<0>(a.S[0]); sm_exp<1>(a.S[0]); sm_exp<2>(a.S[0]); sm_exp<3>(a.S[0]); sm_exp<4>(a.S[0]); sm_exp<5>(a.S[0]); sm_exp<6>(a.S[0]); sm_exp<7>(a.S[0]);
  sm_fin<0>(a.S[0], a.l[0], a.P0); sm_fin<1>(a.S[0], a.l[0], a.P0); sm_fin<2>(a.S[0], a.l[0], a.P0); sm_fin<3>(a.S[0], a.l[0], a.P0);
  sm_fin<4>(a.S[0], a.l[0], a.P0); sm_fin<5>(a.S[0], a.l[0], a.P0); sm_fin<6>(a.S[0], a.l[0], a.P0); sm_fin<7>(a.S[0], a.l[0], a.P0);
  if constexpr (KIND == 1) {
    sm_exp<0>(a.SR); sm_exp<1>(a.SR); sm_exp<2>(a.SR); sm_exp<3>(a.SR);
    sm_fin<0>(a.SR, a.lr, a.PR); sm_fin<1>(a.SR, a.lr, a.PR); sm_fin<2>(a.SR, a.lr, a.PR); sm_fin<3>(a.SR, a.lr, a.PR);
  }
  GAP();
  if constexpr (KIND == 0) {
    _Pragma("unroll") for (int j = 0; j < 4; ++j) dma_nt(a.m0v + SLOT + j * 4096u, a.voff, a.srdV, TOFF(SLOT) + j * 4096u);
    _Pragma("unroll") for (int j = 0; j < 4; ++j) dma_nt(a.m0k + 3u * SLOT + j * 4096u, a.koff, a.srdK, TOFF(3u * SLOT) + j * 4096u);
  }
  GAP();
#pragma nounroll
  for (unsigned so = 0; so < 12u * SLOT; so += 4u * SLOT) {
    step<0, RE, false>(a, qr, so STAMP_PASS);
    step<1, RO, false>(a, qr, so STAMP_PASS);
    step<2, RE, false>(a, qr, so STAMP_PASS);
    step<3, RO, false>(a, qr, so STAMP_PASS);
    if (so == 0) STAMP(3); else if (so == 4u * SLOT) STAMP(4); else STAMP(5);
  }
  if constexpr (KIND == 0) { set_boff(a, fresh_lane()); GAP(); }
  step<0, RE, true>(a, qr, 12u * SLOT STAMP_PASS);
  step<1, RO, true>(a, qr, 12u * SLOT STAMP_PASS);
  step<2, RE, true>(a, qr, 12u * SLOT STAMP_PASS);
  step<3, RO, true>(a, qr, 12u * SLOT STAMP_PASS);
  asm volatile("s_nop 7"); GAP();
  STAMP(6);
  lane = fresh_lane();
  if constexpr (KIND != 0) set_boff(a, lane);
  const unsigned w4 = w & 3u, kh = w >> 2, r16 = lane & 15u, g4 = lane >> 4, q4 = r16 >> 2, p = lane & 3u;
  const unsigned g = h >> 3, j = h & 7u;
  gu64* const flags = (gu64*)(ws + WS_FLAG);
  const __amdgpu_buffer_rsrc_t srdO = __builtin_amdgcn_make_buffer_rsrc((void*)(ws + (size_t)b * 9u * 131072u), (short)0, 9 * 131072, 0x00020000);
  bf16x8 A0[2][2];
  {
    const float l0 = swap32_add(swap16_add(a.l[0])), l1 = swap32_add(swap16_add(a.l[1]));
#pragma unroll
    for (int qt = 0; qt < 2; ++qt)
#pragma unroll
      for (int e = 0; e < 4; ++e) *(LAS f32x4*)(ldsb + PX + w * 8192u + (qt * 4 + e) * 1024u + lane * 16u) = a.O[qt][4 + e];
    if (g4 == 0) *(LAS f32x2*)(ldsb + STATS + (w * 16u + r16) * 8u) = f32x2{l0, l1};
    constexpr int UI = KIND == 2 ? 1 : 0;
    const unsigned uo = 2u * kh + UI;
    if constexpr (KIND != 0) {
      const float lr = swap32_add(swap16_add(a.lr));
#pragma unroll
      for (int i = 0; i < 4; ++i)
        if (i != UI) {
          const unsigned ud = (i + 2u * kh) & 3u;
#pragma unroll
          for (int e = 0; e < 2; ++e) *(LAS f32x4*)(ldsb + RX + (ud * 3u + ((UI - i - 1) & 3)) * 2048u + e * 1024u + lane * 16u) = a.OR[2 * i + e];
        }
      if (g4 == 0) *(LAS float*)(ldsb + STATS + 1024u + (uo * 16u + r16) * 4u) = lr;
    }
    bar();
    const f32x2 lp = *(LAS f32x2*)(ldsb + STATS + ((w ^ 4u) * 16u + r16) * 8u);
    const float inv[2] = {__builtin_amdgcn_rcpf(l0 + lp[1]), __builtin_amdgcn_rcpf(l1 + lp[0])};
#pragma unroll
    for (int qt = 0; qt < 2; ++qt)
#pragma unroll
      for (int ks = 0; ks < 2; ++ks) {
        f32x4 o[2];
#pragma unroll
        for (int e = 0; e < 2; ++e) o[e] = (a.O[qt][2 * ks + e] + *(LAS f32x4*)(ldsb + PX + (w ^ 4u) * 8192u + ((qt ^ 1) * 4 + 2 * ks + e) * 1024u + lane * 16u)) * inv[qt];
        const u32x4 f = {cvtpk(o[0][0], o[0][1]), cvtpk(o[0][2], o[0][3]), cvtpk(o[1][0], o[1][1]), cvtpk(o[1][2], o[1][3])};
        A0[qt][ks] = __builtin_bit_cast(bf16x8, f);
        __builtin_amdgcn_raw_buffer_store_b128(f, srdO, lane * 16u, (2u * w4 + (qt ^ kh)) * 131072u + (h * 4u + 2u * kh + ks) * 1024u, ST_AUX);
      }
    if constexpr (KIND != 0) {
      float L = 0.f; f32x4 o[2] = {a.OR[2 * UI], a.OR[2 * UI + 1]};
#pragma unroll
      for (int i = 0; i < 4; ++i) L += *(LAS float*)(ldsb + STATS + 1024u + (i * 16u + r16) * 4u);
#pragma unroll
      for (int i = 0; i < 3; ++i)
#pragma unroll
        for (int e = 0; e < 2; ++e) o[e] += *(LAS f32x4*)(ldsb + RX + (uo * 3u + i) * 2048u + e * 1024u + lane * 16u);
      const float iv = __builtin_amdgcn_rcpf(L);
      o[0] *= iv; o[1] *= iv;
      const u32x4 f = {cvtpk(o[0][0], o[0][1]), cvtpk(o[0][2], o[0][3]), cvtpk(o[1][0], o[1][1]), cvtpk(o[1][2], o[1][3])};
      __builtin_amdgcn_raw_buffer_store_b128(f, srdO, lane * 16u, 8u * 131072u + (h * 4u + uo) * 1024u, ST_AUX);
      *(LAS u32x4*)(ldsb + QREM + uo * 1024u + lane * 16u) = f;
    }
  }
  STAMP(7);
  waitvm<0>();
  bar();
  if (KIND == 1 && w == 0 && lane == 0) __hip_atomic_store(flags + (b * 32u + h) * 16u, tag, __ATOMIC_RELAXED, __HIP_MEMORY_SCOPE_AGENT);
  STAMP(8);
#pragma unroll
  for (int i = 0; i < 4; ++i) dma_b(a, 2u + ((i + b) & 3u), w);
  unsigned ba[2];
#pragma unroll
  for (int e = 0; e < 2; ++e) ba[e] = ldsb + (64u * kh + 4u * g4 + q4) * 192u + ((((j & 1u) + e) ^ (g4 & 1u)) - e) * 32u + 8u * p;
  auto read_b = [&](int slot, int ks, bf16x8 (&bf)[5]) {
#pragma unroll
    for (int nt = 0; nt < 5; ++nt) { const unsigned ad = ba[nt & 1] + slot * BSZ + ks * 6144 + nt * 32; bf[nt] = cat(ldstr(ad), ldstr(ad + 3072)); }
  };
  f32x4 acc[2][5], accr[5];
#pragma unroll
  for (int nt = 0; nt < 5; ++nt) { acc[0][nt] = kZero4; acc[1][nt] = kZero4; accr[nt] = kZero4; }
  bf16x8 B1[2][5];
  {
#pragma unroll
    for (int ks = 0; ks < 2; ++ks) {
      bf16x8 bf[5];
      read_b(0, ks, bf);
      bf16x8 ar;
      if constexpr (KIND == 1) ar = lds128(ldsb + QREM + (2u * kh + ks) * 1024u + lane * 16u);
#pragma unroll
      for (int nt = 0; nt < 5; ++nt) {
        acc[0][nt] = MFMA16(bf[nt], A0[0][ks], acc[0][nt]); acc[1][nt] = MFMA16(bf[nt], A0[1][ks], acc[1][nt]);
        if constexpr (KIND == 1) accr[nt] = MFMA16(bf[nt], ar, accr[nt]);
      }
    }
    read_b(1, 0, B1[0]); read_b(1, 1, B1[1]);
  }
  bar();
  dma_b(a, 6u, w); dma_b(a, 7u, w);
  if (w == 0) {
    gu64* f = flags + (b * 32u + g * 8u + (lane & 7u)) * 16u;
    for (unsigned spin = 0; spin < (1u << 20); ++spin) {
      if (__all(__hip_atomic_load(f, __ATOMIC_RELAXED, __HIP_MEMORY_SCOPE_AGENT) == tag)) break;
      __builtin_amdgcn_s_sleep(1);
    }
  }
  STAMP(9);
  waitvm<0>();
  bar();
  STAMP(10);
  bf16x8 Af[8][2][2], Ar[8][2];
  auto load_a = [&](int s) {
    const unsigned so = ((g * 8u + ((j + s) & 7u)) * 4u + 2u * kh) * 1024u;
#pragma unroll
    for (int ks = 0; ks < 2; ++ks) {
#pragma unroll
      for (int qt = 0; qt < 2; ++qt) Af[s][qt][ks] = __builtin_bit_cast(bf16x8, __builtin_amdgcn_raw_buffer_load_b128(srdO, lane * 16u + ks * 1024u, (2u * w4 + (qt ^ kh)) * 131072u + so, LD_AUX));
      if (KIND != 0 && (s & 1) == (KIND - 1)) Ar[s][ks] = __builtin_bit_cast(bf16x8, __builtin_amdgcn_raw_buffer_load_b128(srdO, lane * 16u + ks * 1024u, 8u * 131072u + so, LD_AUX));
    }
  };
  load_a(1); load_a(2);
  GAP();
#pragma unroll
  for (int s = 1; s < 8; ++s) {
    bf16x8 Bn[2][5];
    if (s < 7) { read_b((s + 1) % 6, 0, Bn[0]); read_b((s + 1) % 6, 1, Bn[1]); }
#pragma unroll
    for (int ks = 0; ks < 2; ++ks)
#pragma unroll
      for (int nt = 0; nt < 5; ++nt) {
        acc[0][nt] = MFMA16(B1[ks][nt], Af[s][0][ks], acc[0][nt]); acc[1][nt] = MFMA16(B1[ks][nt], Af[s][1][ks], acc[1][nt]);
        if (KIND != 0 && (s & 1) == (KIND - 1)) accr[nt] = MFMA16(B1[ks][nt], Ar[s][ks], accr[nt]);
      }
#pragma unroll
    for (int i = 0; i < 20; ++i) { __builtin_amdgcn_sched_group_barrier(0x008, 1, 0); __builtin_amdgcn_sched_group_barrier(0x100, 1, 0); }
    GAP();
    if (s + 2 < 8) load_a(s + 2);
    GAP();
    if (s < 7) {
#pragma unroll
      for (int ks = 0; ks < 2; ++ks)
#pragma unroll
        for (int nt = 0; nt < 5; ++nt) B1[ks][nt] = Bn[ks][nt];
    }
  }
  STAMP(11);
  bar();
#pragma unroll
  for (int nt = 0; nt < 5; ++nt) *(LAS f32x4*)(ldsb + w * 5120u + nt * 1024u + lane * 16u) = acc[1][nt];
  if constexpr (KIND != 0) {
#pragma unroll
    for (int nt = 0; nt < 5; ++nt) *(LAS f32x4*)(ldsb + 40960u + w4 * 5120u + nt * 1024u + lane * 16u) = accr[nt];
  }
  bar();
  const __amdgpu_buffer_rsrc_t srdM = __builtin_amdgcn_make_buffer_rsrc((void*)(ws + WS_MBOX + (size_t)b * 32u * MBOX), (short)0, 32 * MBOX, 0x00020000);
  {
    const unsigned vo = q4 * (8u * (unsigned)MBOX) + (g4 * 4u + p) * 16u, so = j * (unsigned)MBOX + g * 11520u;
#pragma unroll
    for (int nt = 0; nt < 5; ++nt) {
      const f32x4 v = acc[0][nt] + *(LAS f32x4*)(ldsb + (w ^ 4u) * 5120u + nt * 1024u + lane * 16u);
      __builtin_amdgcn_raw_buffer_store_b128(__builtin_bit_cast(u32x4, v), srdM, vo, so + ((2u * w4 + kh) * 5u + nt) * 256u, ST_AUX);
    }
    if constexpr (KIND == 0) {
      for (unsigned nt = w4; nt < 5u; nt += 4u) {
        f32x4 v = kZero4;
#pragma unroll
        for (int i = 0; i < 4; ++i) v += *(LAS f32x4*)(ldsb + 40960u + i * 5120u + nt * 1024u + lane * 16u);
        __builtin_amdgcn_raw_buffer_store_b128(__builtin_bit_cast(u32x4, v), srdM, vo, so + (40u + nt) * 256u, ST_AUX);
      }
    }
  }
  STAMP(12);
  waitvm<0>();
  bar();
  if (w == 0) {
    if (lane == 0) __hip_atomic_store(flags + (256u + b * 32u + h) * 16u, tag, __ATOMIC_RELAXED, __HIP_MEMORY_SCOPE_AGENT);
    STAMP(13);
    gu64* f = flags + (256u + b * 32u + (lane & 3u) * 8u + j) * 16u;
    for (unsigned spin = 0; spin < (1u << 20); ++spin) {
      if (__all(__hip_atomic_load(f, __ATOMIC_RELAXED, __HIP_MEMORY_SCOPE_AGENT) == tag)) break;
      __builtin_amdgcn_s_sleep(1);
    }
    if (h == 0 && lane == 0) __hip_atomic_store(flags + (512u + b) * 16u, tag, __ATOMIC_RELAXED, __HIP_MEMORY_SCOPE_AGENT);
  }
  bar();
  STAMP(14);
#pragma unroll
  for (int k = 0; k < 2; ++k) {
    const unsigned u = w * 64u + lane + 512u * k;
    if (k == 0 || w < 4u) {
      f32x4 v[4];
#pragma unroll
      for (int i = 0; i < 4; ++i) v[i] = __builtin_bit_cast(f32x4, __builtin_amdgcn_raw_buffer_load_b128(srdM, u * 16u, h * (unsigned)MBOX + i * 11520u, LD_AUX));
      const f32x4 r = (v[0] + v[1]) + (v[2] + v[3]);
      const unsigned t = u >> 4, mt = t / 5u, nt = t - 5u * mt;
      if (u < 720u) *(u32x2*)(out + (size_t)(b * SQ + 16u * mt + 4u * g + (u & 3u)) * DM + 80u * j + 16u * nt + ((u >> 2) & 3u) * 4u) = u32x2{cvtpk(r[0], r[1]), cvtpk(r[2], r[3])};
    }
  }
  STAMP_END;
}

__global__ __launch_bounds__(512, 2) void attn_kernel(const u16* __restrict__ x, const u16* __restrict__ kc, const u16* __restrict__ vc,
                                                      const u16* __restrict__ wq, const u16* __restrict__ wo, char* ws, u16* __restrict__ out, unsigned long long* __restrict__ stamps) {
  extern __shared__ __attribute__((aligned(16))) char lds[];
  const unsigned ldsb = (unsigned)(size_t)(LAS char*)lds;
  const unsigned lane = threadIdx.x & 63u;
  const unsigned w = rfl(threadIdx.x >> 6);
  const unsigned bid = blockIdx.x, xg = bid & 7u, wi = bid >> 3;
  const unsigned h = (xg & 3u) * 8u + (wi & 7u), b = (xg >> 2) * 4u + (wi >> 3);
  STAMP_DECL;
  STAMP(0);
  Att a;
  a.srdK = __builtin_amdgcn_make_buffer_rsrc((void*)(kc + (size_t)(b * NH + h) * SK * DH), (short)0, SK * DH * 2, 0x00020000);
  a.srdV = __builtin_amdgcn_make_buffer_rsrc((void*)(vc + (size_t)(b * NH + h) * SK * DH), (short)0, SK * DH * 2, 0x00020000);
  const unsigned w4 = w & 3u, kh = w >> 2;
  const bool loader = ((w + 2u) & 4u) != 0u;
  const unsigned r16 = lane & 15u, g4 = lane >> 4, q4 = r16 >> 2, p = lane & 3u;
  const unsigned prow = 4u * w4 + g4;
  a.koff = prow * 256u + ((r16 ^ prow) << 4);
  a.voff = prow * 256u + ((r16 ^ ((g4 << 2) | ((w4 & 1u) << 1))) << 4);
  a.rot = ((bid * 5u + (bid >> 4)) & 15u) * SLOT;
  a.m0k = ldsb + KR + w4 * 1024u; a.m0v = ldsb + VR + w4 * 1024u;
  auto dma_k = [&](unsigned t) { _Pragma("unroll") for (int j = 0; j < 4; ++j) dma_nt(a.m0k + (t & 3u) * SLOT + j * 4096u, a.koff, a.srdK, TOFF(t * SLOT) + j * 4096u); };
  auto dma_v = [&](unsigned t) { _Pragma("unroll") for (int j = 0; j < 4; ++j) dma_nt(a.m0v + (t & 3u) * SLOT + j * 4096u, a.voff, a.srdV, TOFF(t * SLOT) + j * 4096u); };
  const unsigned wsw = (q4 << 2) | ((g4 & 1u) << 1);
  unsigned long long tag = 0;
  if (w == 0) {
    const unsigned long long e = __hip_atomic_load((gu64*)(ws + WS_FLAG) + (512u + b) * 16u, __ATOMIC_RELAXED, __HIP_MEMORY_SCOPE_AGENT) + 1ull;
    tag = ((unsigned long long)rfl((unsigned)(e >> 32)) << 32) | rfl((unsigned)e);
  }

  if (!loader) {
    const unsigned qh = w4 & 1u, dh = w4 >> 1;
    f32x4 acc[4][4];
#pragma unroll
    for (int dt = 0; dt < 4; ++dt)
#pragma unroll
      for (int qt = 0; qt < 4; ++qt) acc[dt][qt] = kZero4;
    unsigned xa[2], wa[4];
#pragma unroll
    for (int s = 0; s < 2; ++s) xa[s] = ldsb + XS0 + (64u * qh + r16) * 128u + (((4u * s + g4) ^ (r16 >> 1)) << 4);
#pragma unroll
    for (int dt = 0; dt < 4; ++dt) wa[dt] = ldsb + WBASE + (8u * g4 + q4) * 256u + (((8u * dh + 2u * dt + (p >> 1)) ^ wsw) << 4) + 8u * (p & 1u);
    bf16x8 xf[2][4], wf[2][4];
#pragma unroll
    for (int n = 0; n <= 20; ++n) {
      const int sg = n >> 1, s = n & 1;
      if (n < 20 && s == 0) { bar(); GAP(); }
#pragma unroll
      for (int m = 0; m < 16; ++m) {
        if (n > 0) acc[m >> 2][m & 3] = MFMA16(wf[s ^ 1][m >> 2], xf[s ^ 1][m & 3], acc[m >> 2][m & 3]);
        if (n < 20 && m < 8) {
          const int j = m >> 1;
          if (m & 1) xf[s][j] = lds128(xa[s] + (sg % 3) * XSZ + j * 2048);
          else { const unsigned ad = wa[j] + wslot(sg) + s * 8192; wf[s][j] = cat(ldstr(ad), ldstr(ad + 1024)); }
        }
        GAP();
      }
    }
#pragma unroll
    for (int dt = 0; dt < 4; ++dt)
#pragma unroll
      for (int qt = 0; qt < 4; ++qt) {
        u32x2 q = {cvtpk(acc[dt][qt][0] * QSCALE, acc[dt][qt][1] * QSCALE), cvtpk(acc[dt][qt][2] * QSCALE, acc[dt][qt][3] * QSCALE)};
        *(LAS u32x2*)(ldsb + QI + (4u * qh + qt) * 4096u + (2u * dh + (dt >> 1)) * 1024u + ((2u * (dt & 1) + (g4 >> 1)) * 16u + r16) * 16u + (g4 & 1u) * 8u) = q;
      }
    STAMP(1);
    bar();
  } else {
    const __amdgpu_buffer_rsrc_t srdX = __builtin_amdgcn_make_buffer_rsrc((void*)(x + (size_t)b * SQ * DM), (short)0, SQ * DM * 2, 0x00020000);
    const __amdgpu_buffer_rsrc_t srdW = __builtin_amdgcn_make_buffer_rsrc((void*)(wq + h * DH), (short)0, (DM * HD - h * DH) * 2, 0x00020000);
    const unsigned xoff = (8u * w4 + (lane >> 3)) * (DM * 2u) + (((lane & 7u) ^ (4u * (w4 & 1u) + g4)) << 4);
    const unsigned woff = prow * (HD * 2u) + ((r16 ^ ((g4 << 2) | (((w4 >> 1) & 1u) << 1))) << 4);
    const unsigned rot = (wi * 3u) % 10u;
    auto dma_stage = [&](int sg) {
      unsigned kk = sg + rot; kk = kk >= 10u ? kk - 10u : kk;
      const unsigned wsl = WBASE + wslot(sg), xsl = XS0 + (sg % 3) * XSZ;
#pragma unroll
      for (int i = 0; i < 4; ++i) {
        DMAXW(ldsb + wsl + (w4 + 4u * i) * 1024u, woff, srdW, (kk * 64u + 16u * i) * (HD * 2u));
        DMAXW(ldsb + xsl + (w4 + 4u * i) * 1024u, xoff, srdX, kk * 128u + 32u * i * (DM * 2u));
      }
      if (w4 < 2) DMAXW(ldsb + xsl + (w4 + 16u) * 1024u, xoff, srdX, kk * 128u + 128u * (DM * 2u));
    };
    dma_stage(0); dma_stage(1);
    f32x4 acc[2] = {kZero4, kZero4};
    unsigned xa[2], wa[2];
#pragma unroll
    for (int s = 0; s < 2; ++s) xa[s] = ldsb + XS0 + (128u + r16) * 128u + (((4u * s + g4) ^ (r16 >> 1)) << 4);
#pragma unroll
    for (int e = 0; e < 2; ++e) wa[e] = ldsb + WBASE + (8u * g4 + q4) * 256u + (((4u * w4 + 2u * e + (p >> 1)) ^ wsw) << 4) + 8u * (p & 1u);
#pragma unroll
    for (int sg = 0; sg < 10; ++sg) {
      if (sg < 9) { if (w4 < 2) waitvm<9>(); else waitvm<8>(); }
      else waitvm<4>();
      bar();
      bf16x8 xf[2], wf[2][2];
#pragma unroll
      for (int s = 0; s < 2; ++s) {
        xf[s] = lds128(xa[s] + (sg % 3) * XSZ);
#pragma unroll
        for (int e = 0; e < 2; ++e) { const unsigned ad = wa[e] + wslot(sg) + s * 8192; wf[s][e] = cat(ldstr(ad), ldstr(ad + 1024)); }
      }
      GAP();
      if (sg + 2 < 10) dma_stage(sg + 2);
      if (sg == 8) dma_k(0);
      if (sg == 9) dma_k(1);
      GAP();
#pragma unroll
      for (int s = 0; s < 2; ++s)
#pragma unroll
        for (int e = 0; e < 2; ++e) acc[e] = MFMA16(wf[s][e], xf[s], acc[e]);
    }
#pragma unroll
    for (int e = 0; e < 2; ++e) {
      u32x2 q = {cvtpk(acc[e][0] * QSCALE, acc[e][1] * QSCALE), cvtpk(acc[e][2] * QSCALE, acc[e][3] * QSCALE)};
      *(LAS u32x2*)(ldsb + QREM + w4 * 1024u + ((2u * e + (g4 >> 1)) * 16u + r16) * 16u + (g4 & 1u) * 8u) = q;
    }
    STAMP(1);
    waitvm<4>();
    bar();
    dma_v(0);
  }
  STAMP(2);
#pragma unroll
  for (int s = 0; s < 4; ++s) a.ka[s] = ldsb + KR + (32u * kh + r16) * 256u + (((4u * s + g4) ^ r16) << 4);
#pragma unroll
  for (int dt = 0; dt < 8; ++dt) a.va[dt] = ldsb + VR + (32u * kh + 4u * g4 + q4) * 256u + (((2u * ((dt + 4u * kh) & 7u) + (p >> 1)) ^ wsw) << 4) + 8u * (p & 1u);
  a.qra = ldsb + QREM + lane * 16u;
  a.ldsb = ldsb; a.j = h & 7u; a.w4 = w4;
  a.srdB = __builtin_amdgcn_make_buffer_rsrc((void*)((const char*)wo + (size_t)(h >> 3) * 1024u * DM * 2u + (h & 7u) * 160u - (h & 1u) * 32u), (short)0, 1023 * DM * 2 + 192, 0x00020000);
  if (loader) attention<0>(a, ldsb, lane, w, b, h, ws, out, tag STAMP_PASS);
  else if (w & 1u) attention<2>(a, ldsb, lane, w, b, h, ws, out, tag STAMP_PASS);
  else attention<1>(a, ldsb, lane, w, b, h, ws, out, tag STAMP_PASS);

}

extern "C" void kernel_launch(void* const* d_in, const int* in_sizes, int n_in, void* d_out, int out_size, void* d_ws, size_t ws_size, hipStream_t stream) {
  const u16* x = (const u16*)d_in[0]; const u16* k = (const u16*)d_in[1]; const u16* v = (const u16*)d_in[2];
  const u16* wq = (const u16*)d_in[3]; const u16* wo = (const u16*)d_in[4];
  unsigned long long* stamps = (unsigned long long*)((char*)d_ws + (32u << 20));
  (void)hipFuncSetAttribute((const void*)attn_kernel, hipFuncAttributeMaxDynamicSharedMemorySize, LDS_A);
  attn_kernel<<<256, 512, LDS_A, stream>>>(x, k, v, wq, wo, (char*)d_ws, (u16*)d_out, stamps);
}
```

```cpp
#include <hip/hip_runtime.h>
#include <hip/hip_bf16.h>
#include <utility>

typedef unsigned short u16;
typedef float f32x16 __attribute__((ext_vector_type(16)));
typedef float f32x4 __attribute__((ext_vector_type(4)));
typedef float f32x2 __attribute__((ext_vector_type(2)));
typedef __bf16 bf16x8 __attribute__((ext_vector_type(8)));
typedef __bf16 bf16x4 __attribute__((ext_vector_type(4)));
typedef __bf16 bf16x2 __attribute__((ext_vector_type(2)));
typedef unsigned u32x4 __attribute__((ext_vector_type(4)));
typedef unsigned u32x2 __attribute__((ext_vector_type(2)));
#define LAS __attribute__((address_space(3)))
#define DI __device__ __forceinline__

constexpr int NB = 8, SQ = 144, DM = 640, NH = 32, SK = 1024, DH = 128;
constexpr int HD = NH * DH;
constexpr float QSCALE = 0.12751743082f;
constexpr float THR = 8.0f;

#define STAMPS 0

constexpr unsigned KR = 0, VR = 65536, SLOT = 16384;
constexpr unsigned WBASE = 32768;
constexpr unsigned XS0 = 98304, XSZ = 18432;
constexpr unsigned QI = XS0 + XSZ;
constexpr unsigned QREM = 159744;
constexpr unsigned STATS = 155648;
constexpr unsigned PX = 49152, RX = 131072;
constexpr unsigned BSZ = 24576;
constexpr size_t WS_MBOX = 9437184, MBOX = 46080;
constexpr size_t WS_FLAG = WS_MBOX + 256 * MBOX;
typedef __attribute__((address_space(1))) unsigned long long gu64;
constexpr unsigned LDS_A = 163840;
constexpr unsigned wslot(int sg) { return sg % 3 == 0 ? 16384u : sg % 3 == 1 ? 0u : 49152u; }

DI unsigned rfl(unsigned x) { return __builtin_amdgcn_readfirstlane(x); }
DI unsigned cvtpk(float lo, float hi) { f32x2 v = {lo, hi}; bf16x2 b = __builtin_convertvector(v, bf16x2); return __builtin_bit_cast(unsigned, b); }
DI bf16x8 lds128(unsigned a) { return *(LAS bf16x8*)a; }
DI bf16x4 ldstr(unsigned a) { return __builtin_amdgcn_ds_read_tr16_b64_v4bf16((LAS bf16x4*)a); }
DI bf16x8 cat(bf16x4 a, bf16x4 b) { return __builtin_shufflevector(a, b, 0, 1, 2, 3, 4, 5, 6, 7); }
#define MFMA32(a, b, c) __builtin_amdgcn_mfma_f32_32x32x16_bf16((a), (b), (c), 0, 0, 0)
#define MFMA16(a, b, c) __builtin_amdgcn_mfma_f32_16x16x32_bf16((a), (b), (c), 0, 0, 0)
template <int N> DI void waitvm() { asm volatile("s_waitcnt vmcnt(%0)" ::"i"(N) : "memory"); }
DI void bar() { asm volatile("s_waitcnt lgkmcnt(0)\n\ts_barrier" ::: "memory"); }
DI void dma(unsigned m0, unsigned voff, __amdgpu_buffer_rsrc_t srd, unsigned soff) {
  asm volatile("s_mov_b32 m0, %0\n\ts_nop 0\n\tbuffer_load_dwordx4 %1, %2, %3 offen lds" ::"s"(m0), "v"(voff), "s"(srd), "s"(soff) : "memory");
}
DI void dma_nt(unsigned m0, unsigned voff, __amdgpu_buffer_rsrc_t srd, unsigned soff) {
  asm volatile("s_mov_b32 m0, %0\n\ts_nop 0\n\tbuffer_load_dwordx4 %1, %2, %3 offen nt lds" ::"s"(m0), "v"(voff), "s"(srd), "s"(soff) : "memory");
}
DI float swap32_max(float x) { auto r = __builtin_amdgcn_permlane32_swap(__float_as_uint(x), __float_as_uint(x), false, false); return fmaxf(__uint_as_float(r[0]), __uint_as_float(r[1])); }
DI float swap32_add(float x) { auto r = __builtin_amdgcn_permlane32_swap(__float_as_uint(x), __float_as_uint(x), false, false); return __uint_as_float(r[0]) + __uint_as_float(r[1]); }
DI float swap16_max(float x) { auto r = __builtin_amdgcn_permlane16_swap(__float_as_uint(x), __float_as_uint(x), false, false); return fmaxf(__uint_as_float(r[0]), __uint_as_float(r[1])); }
DI float swap16_add(float x) { auto r = __builtin_amdgcn_permlane16_swap(__float_as_uint(x), __float_as_uint(x), false, false); return __uint_as_float(r[0]) + __uint_as_float(r[1]); }
DI float max3f(float a, float b, float c) { return __builtin_fmaxf(__builtin_fmaxf(a, b), c); }

#define STAMP_DECL
#define STAMP_ARG
#define STAMP_PASS
#define STAMP_END
#define STAMP(i) do {} while (0)
#define STAMP_OUT(lo, hi) do {} while (0)

DI float fadd(float a, float b) { float r; asm("v_add_f32_e32 %0, %1, %2" : "=v"(r) : "v"(a), "v"(b)); return r; }
#define GAP() __builtin_amdgcn_sched_barrier(0)
const f32x4 kZero4 = {0.f, 0.f, 0.f, 0.f};

#define TOFF(x) (x)
#define FSTAMP(i) do {} while (0)
#define DMAXW dma
#define DMAB dma
#define ST_AUX 16
#define LD_AUX 16
constexpr unsigned CODE_LINES = 608;
struct Att {
  f32x4 O[2][8], OR[8];
  bf16x8 Q[2][4];
  bf16x8 F[8];
  f32x4 S[2][2], SR[2];
  f32x4 T[2];
  u32x4 P0, P0n, P1, PR;
  float l[2], lr;
  unsigned ka[4], va[8], qra;
  unsigned koff, voff, m0k, m0v, rot;
  __amdgpu_buffer_rsrc_t srdK, srdV, srdB;
  unsigned boff[3], ldsb, j, w4;
};
DI unsigned fresh_lane() { unsigned l; asm volatile("v_mbcnt_lo_u32_b32 %0, -1, 0\n\tv_mbcnt_hi_u32_b32 %0, -1, %0" : "=v"(l)); return l; }
DI void set_boff(Att& a, unsigned lane) {
#pragma unroll
  for (int i = 0; i < 3; ++i) { const unsigned qd = 16u * i + (lane >> 2), row = qd / 3u; a.boff[i] = row * (DM * 2u) + (qd - 3u * row) * 64u + (((lane & 3u) ^ (((row >> 2) & 1u) << 1)) << 4); }
}
DI void dma_b(const Att& a, unsigned s, unsigned rb) {
  const unsigned m = a.ldsb + (s >= 6u ? s - 6u : s) * BSZ + rb * 3072u, so = ((((a.j + s) & 7u) << 7) + rb * 16u) * (DM * 2u);
#pragma unroll
  for (int i = 0; i < 3; ++i) DMAB(m + i * 1024u, a.boff[i], a.srdB, so);
}
DI void mma(f32x4& d, bf16x8 a, bf16x8 b) { asm volatile("v_mfma_f32_16x16x32_bf16 %0, %1, %2, %0" : "+v"(d) : "v"(a), "v"(b)); GAP(); }
DI void mma0(f32x4& d, bf16x8 a, bf16x8 b) { asm volatile("v_mfma_f32_16x16x32_bf16 %0, %1, %2, 0" : "=&v"(d) : "v"(a), "v"(b)); GAP(); }
DI void mmap(f32x4& d, bf16x8 a, u32x4 b) { asm volatile("v_mfma_f32_16x16x32_bf16 %0, %1, %2, %0" : "+v"(d) : "v"(a), "v"(b)); GAP(); }
template <int E> DI void sm_exp(f32x4 (&s)[2]) { s[E >> 2][E & 3] = __builtin_amdgcn_exp2f(s[E >> 2][E & 3]); GAP(); }
template <int E> DI void sm_fin(f32x4 (&s)[2], float& l, u32x4& p) {
  if constexpr (E & 1) { p[E >> 1] = cvtpk(s[E >> 2][(E & 3) - 1], s[E >> 2][E & 3]); GAP(); }
  l = fadd(l, s[E >> 2][E & 3]); GAP();
}
template <int SL, int ROLE, bool FIN, int M> DI void gapA(Att& a, bf16x8 (&qr)[4], unsigned so STAMP_ARG) {
  constexpr int kt = M >> 3, s = (M >> 1) & 3, qt = M & 1;
  constexpr bool LAST = FIN && SL == 3;
  if constexpr (!LAST) {
    if constexpr (s == 0) mma0(a.S[qt][kt], a.F[kt * 4 + s], a.Q[qt][s]); else mma(a.S[qt][kt], a.F[kt * 4 + s], a.Q[qt][s]);
  }
  if constexpr (M == 3) {
    if constexpr (!FIN && SL == 1) FSTAMP(12);
    if constexpr (ROLE == 0) waitvm<(!FIN || SL == 0) ? 8 : SL == 1 ? 4 : SL == 2 ? 10 : 12>();
    if constexpr (!FIN && SL == 1) FSTAMP(10);
    bar(); GAP();
    if constexpr (!FIN && SL == 1) FSTAMP(11);
  }
  if constexpr (!(M & 1)) sm_exp<(M >> 1)>(a.T); else { if constexpr (LAST) asm volatile("s_nop 0"); sm_fin<(M >> 1)>(a.T, a.l[1], a.P1); }
  if constexpr (!LAST && ROLE == 1 && qt == 1) {
    if constexpr (s == 0) mma0(a.SR[kt], a.F[kt * 4 + s], qr[s]); else mma(a.SR[kt], a.F[kt * 4 + s], qr[s]);
  }
  if constexpr (M >= 3 && (M & 1)) {
    if constexpr (M == 3) { a.F[0] = cat(ldstr(a.va[0] + SL * SLOT), ldstr(a.va[0] + SL * SLOT + 4096)); GAP(); }
    constexpr int j = M >> 1;
    a.F[j] = cat(ldstr(a.va[j] + SL * SLOT), ldstr(a.va[j] + SL * SLOT + 4096)); GAP();
  }
  if constexpr (ROLE == 2 && (M & 3) == 1) sm_exp<4 + (M >> 2)>(a.SR);
  if constexpr (ROLE == 2 && (M & 3) == 2) sm_fin<4 + (M >> 2)>(a.SR, a.lr, a.PR);
  if constexpr (ROLE == 0 && (!FIN || SL <= 1) && M >= 6 && M <= 12 && !(M & 1)) {
    constexpr int j = (M - 6) >> 1;
    dma_nt(a.m0v + ((SL + 2) & 3) * SLOT + j * 4096u, a.voff, a.srdV, TOFF(so + (SL + 2u) * SLOT) + j * 4096u); GAP();
  }
}
template <int SL, int ROLE, bool FIN, int M> DI void gapB(Att& a, bf16x8 (&qr)[4], unsigned so) {
  constexpr int dt = M >> 1, qt = M & 1;
  constexpr bool LAST = FIN && SL == 3;
  mmap(a.O[qt][dt], a.F[dt], qt ? a.P1 : a.P0);
  if constexpr (!LAST) { if constexpr (!(M & 1)) sm_exp<(M >> 1)>(a.S[0]); else sm_fin<(M >> 1)>(a.S[0], a.l[0], a.P0n); }
  if constexpr (ROLE == 2 && qt == 1) mmap(a.OR[dt], a.F[dt], a.PR);
  if constexpr (!LAST) {
    if constexpr ((M & 1) && !(FIN && SL == 2)) { a.F[dt] = lds128(a.ka[dt & 3] + ((SL + 2) & 3) * SLOT + (dt >> 2) * 4096); GAP(); }
    if constexpr (ROLE == 2 && M >= 8 && !(M & 1) && !(FIN && SL == 2)) { qr[(M - 8) >> 1] = lds128(a.qra + ((M - 8) >> 1) * 1024); GAP(); }
    if constexpr (ROLE == 1 && (M & 3) == 1) sm_exp<(M >> 2)>(a.SR);
    if constexpr (ROLE == 1 && (M & 3) == 2) sm_fin<(M >> 2)>(a.SR, a.lr, a.PR);
    if constexpr (ROLE == 0 && FIN && SL >= 1 && (M & 7) == 2) { dma_b(a, SL - 1, 2u * a.w4 + (M >> 3)); GAP(); }
    if constexpr (ROLE == 0 && !FIN && (M & 3) == 2) {
      constexpr int j = M >> 2;
      dma_nt(a.m0k + SL * SLOT + j * 4096u, a.koff, a.srdK, TOFF(so + (SL + 4u) * SLOT) + j * 4096u); GAP();
    }
  }
}
template <int SL, int ROLE, bool FIN, int... Ms> DI void step_(Att& a, bf16x8 (&qr)[4], unsigned so, std::integer_sequence<int, Ms...> STAMP_ARG) {
  (gapA<SL, ROLE, FIN, Ms>(a, qr, so STAMP_PASS), ...);
  if constexpr (!FIN && SL == 1) FSTAMP(14);
  if constexpr (FIN && SL == 3) asm volatile("s_nop 1");
  (gapB<SL, ROLE, FIN, Ms>(a, qr, so), ...);
  if constexpr (!FIN && SL == 1) FSTAMP(15);
  a.T[0] = a.S[1][0]; a.T[1] = a.S[1][1]; a.P0 = a.P0n;
}
template <int SL, int ROLE, bool FIN> DI void step(Att& a, bf16x8 (&qr)[4], unsigned so STAMP_ARG) { step_<SL, ROLE, FIN>(a, qr, so, std::make_integer_sequence<int, 16>{} STAMP_PASS); }

template <int KIND> DI void attention(Att& a, unsigned ldsb, unsigned lane, unsigned w, unsigned b, unsigned h, char* ws, u16* __restrict__ out, unsigned long long tag STAMP_ARG) {
  constexpr int RE = KIND == 0 ? 0 : KIND == 1 ? 2 : 1, RO = KIND == 0 ? 0 : KIND == 1 ? 1 : 2;
  bf16x8 qr[4];
  const unsigned qg = w & 3u;
#pragma unroll
  for (int j = 0; j < 8; ++j) a.F[j] = lds128(a.ka[j & 3] + (j >> 2) * 4096);
#pragma unroll
  for (int qt = 0; qt < 2; ++qt)
#pragma unroll
    for (int s = 0; s < 4; ++s) a.Q[qt][s] = lds128(ldsb + QI + (2u * qg + (qt ^ (w >> 2))) * 4096u + s * 1024u + lane * 16u);
  if constexpr (KIND != 0) {
#pragma unroll
    for (int s = 0; s < 4; ++s) qr[s] = lds128(a.qra + s * 1024);
  }
  if constexpr (KIND == 0) waitvm<4>();
  bar();
  if constexpr (KIND == 0) {
    _Pragma("unroll") for (int j = 0; j < 4; ++j) dma_nt(a.m0k + 2u * SLOT + j * 4096u, a.koff, a.srdK, TOFF(2u * SLOT) + j * 4096u);
  }
  GAP();
#pragma unroll
  for (int kt = 0; kt < 2; ++kt)
#pragma unroll
    for (int s = 0; s < 4; ++s) {
      if (s == 0) mma0(a.S[0][kt], a.F[kt * 4 + s], a.Q[0][s]); else mma(a.S[0][kt], a.F[kt * 4 + s], a.Q[0][s]);
      if (s == 0) mma0(a.T[kt], a.F[kt * 4 + s], a.Q[1][s]); else mma(a.T[kt], a.F[kt * 4 + s], a.Q[1][s]);
      if constexpr (KIND == 1) { if (s == 0) mma0(a.SR[kt], a.F[kt * 4 + s], qr[s]); else mma(a.SR[kt], a.F[kt * 4 + s], qr[s]); }
      a.F[kt * 4 + s] = lds128(a.ka[s] + SLOT + kt * 4096); GAP();
    }
#pragma unroll
  for (int dt = 0; dt < 8; ++dt) { a.O[0][dt] = kZero4; a.O[1][dt] = kZero4; a.OR[dt] = kZero4; }
  a.l[0] = a.l[1] = a.lr = 0.f;
  asm volatile("s_nop 7"); GAP();
  sm_exp<0>(a.S[0]); sm_exp<1>(a.S[0]); sm_exp<2>(a.S[0]); sm_exp<3>(a.S[0]); sm_exp<4>(a.S[0]); sm_exp<5>(a.S[0]); sm_exp<6>(a.S[0]); sm_exp<7>(a.S[0]);
  sm_fin<0>(a.S[0], a.l[0], a.P0); sm_fin<1>(a.S[0], a.l[0], a.P0); sm_fin<2>(a.S[0], a.l[0], a.P0); sm_fin<3>(a.S[0], a.l[0], a.P0);
  sm_fin<4>(a.S[0], a.l[0], a.P0); sm_fin<5>(a.S[0], a.l[0], a.P0); sm_fin<6>(a.S[0], a.l[0], a.P0); sm_fin<7>(a.S[0], a.l[0], a.P0);
  if constexpr (KIND == 1) {
    sm_exp<0>(a.SR); sm_exp<1>(a.SR); sm_exp<2>(a.SR); sm_exp<3>(a.SR);
    sm_fin<0>(a.SR, a.lr, a.PR); sm_fin<1>(a.SR, a.lr, a.PR); sm_fin<2>(a.SR, a.lr, a.PR); sm_fin<3>(a.SR, a.lr, a.PR);
  }
  GAP();
  if constexpr (KIND == 0) {
    _Pragma("unroll") for (int j = 0; j < 4; ++j) dma_nt(a.m0v + SLOT + j * 4096u, a.voff, a.srdV, TOFF(SLOT) + j * 4096u);
    _Pragma("unroll") for (int j = 0; j < 4; ++j) dma_nt(a.m0k + 3u * SLOT + j * 4096u, a.koff, a.srdK, TOFF(3u * SLOT) + j * 4096u);
  }
  GAP();
#pragma nounroll
  for (unsigned so = 0; so < 12u * SLOT; so += 4u * SLOT) {
    step<0, RE, false>(a, qr, so STAMP_PASS);
    step<1, RO, false>(a, qr, so STAMP_PASS);
    step<2, RE, false>(a, qr, so STAMP_PASS);
    step<3, RO, false>(a, qr, so STAMP_PASS);
    if (so == 0) STAMP(3); else if (so == 4u * SLOT) STAMP(4); else STAMP(5);
  }
  if constexpr (KIND == 0) { set_boff(a, fresh_lane()); GAP(); }
  step<0, RE, true>(a, qr, 12u * SLOT STAMP_PASS);
  step<1, RO, true>(a, qr, 12u * SLOT STAMP_PASS);
  step<2, RE, true>(a, qr, 12u * SLOT STAMP_PASS);
  step<3, RO, true>(a, qr, 12u * SLOT STAMP_PASS);
  asm volatile("s_nop 7"); GAP();
  STAMP(6);
  lane = fresh_lane();
  if constexpr (KIND != 0) set_boff(a, lane);
  const unsigned w4 = w & 3u, kh = w >> 2, r16 = lane & 15u, g4 = lane >> 4, q4 = r16 >> 2, p = lane & 3u;
  const unsigned g = h >> 3, j = h & 7u;
  gu64* const flags = (gu64*)(ws + WS_FLAG);
  const __amdgpu_buffer_rsrc_t srdO = __builtin_amdgcn_make_buffer_rsrc((void*)(ws + (size_t)b * 9u * 131072u), (short)0, 9 * 131072, 0x00020000);
  bf16x8 A0[2][2];
  {
    const float l0 = swap32_add(swap16_add(a.l[0])), l1 = swap32_add(swap16_add(a.l[1]));
#pragma unroll
    for (int qt = 0; qt < 2; ++qt)
#pragma unroll
      for (int e = 0; e < 4; ++e) *(LAS f32x4*)(ldsb + PX + w * 8192u + (qt * 4 + e) * 1024u + lane * 16u) = a.O[qt][4 + e];
    if (g4 == 0) *(LAS f32x2*)(ldsb + STATS + (w * 16u + r16) * 8u) = f32x2{l0, l1};
    constexpr int UI = KIND == 2 ? 1 : 0;
    const unsigned uo = 2u * kh + UI;
    if constexpr (KIND != 0) {
      const float lr = swap32_add(swap16_add(a.lr));
#pragma unroll
      for (int i = 0; i < 4; ++i)
        if (i != UI) {
          const unsigned ud = (i + 2u * kh) & 3u;
#pragma unroll
          for (int e = 0; e < 2; ++e) *(LAS f32x4*)(ldsb + RX + (ud * 3u + ((UI - i - 1) & 3)) * 2048u + e * 1024u + lane * 16u) = a.OR[2 * i + e];
        }
      if (g4 == 0) *(LAS float*)(ldsb + STATS + 1024u + (uo * 16u + r16) * 4u) = lr;
    }
    bar();
    const f32x2 lp = *(LAS f32x2*)(ldsb + STATS + ((w ^ 4u) * 16u + r16) * 8u);
    const float inv[2] = {__builtin_amdgcn_rcpf(l0 + lp[1]), __builtin_amdgcn_rcpf(l1 + lp[0])};
#pragma unroll
    for (int qt = 0; qt < 2; ++qt)
#pragma unroll
      for (int ks = 0; ks < 2; ++ks) {
        f32x4 o[2];
#pragma unroll
        for (int e = 0; e < 2; ++e) o[e] = (a.O[qt][2 * ks + e] + *(LAS f32x4*)(ldsb + PX + (w ^ 4u) * 8192u + ((qt ^ 1) * 4 + 2 * ks + e) * 1024u + lane * 16u)) * inv[qt];
        const u32x4 f = {cvtpk(o[0][0], o[0][1]), cvtpk(o[0][2], o[0][3]), cvtpk(o[1][0], o[1][1]), cvtpk(o[1][2], o[1][3])};
        A0[qt][ks] = __builtin_bit_cast(bf16x8, f);
        __builtin_amdgcn_raw_buffer_store_b128(f, srdO, lane * 16u, (2u * w4 + (qt ^ kh)) * 131072u + (h * 4u + 2u * kh + ks) * 1024u, ST_AUX);
      }
    if constexpr (KIND != 0) {
      float L = 0.f; f32x4 o[2] = {a.OR[2 * UI], a.OR[2 * UI + 1]};
#pragma unroll
      for (int i = 0; i < 4; ++i) L += *(LAS float*)(ldsb + STATS + 1024u + (i * 16u + r16) * 4u);
#pragma unroll
      for (int i = 0; i < 3; ++i)
#pragma unroll
        for (int e = 0; e < 2; ++e) o[e] += *(LAS f32x4*)(ldsb + RX + (uo * 3u + i) * 2048u + e * 1024u + lane * 16u);
      const float iv = __builtin_amdgcn_rcpf(L);
      o[0] *= iv; o[1] *= iv;
      const u32x4 f = {cvtpk(o[0][0], o[0][1]), cvtpk(o[0][2], o[0][3]), cvtpk(o[1][0], o[1][1]), cvtpk(o[1][2], o[1][3])};
      __builtin_amdgcn_raw_buffer_store_b128(f, srdO, lane * 16u, 8u * 131072u + (h * 4u + uo) * 1024u, ST_AUX);
      *(LAS u32x4*)(ldsb + QREM + uo * 1024u + lane * 16u) = f;
    }
  }
  STAMP(7);
  waitvm<0>();
  bar();
  if (KIND == 1 && w == 0 && lane == 0) __hip_atomic_store(flags + (b * 32u + h) * 16u, tag, __ATOMIC_RELAXED, __HIP_MEMORY_SCOPE_AGENT);
  STAMP(8);
#pragma unroll
  for (int i = 0; i < 4; ++i) dma_b(a, 2u + ((i + b) & 3u), w);
  unsigned ba[2];
#pragma unroll
  for (int e = 0; e < 2; ++e) ba[e] = ldsb + (64u * kh + 4u * g4 + q4) * 192u + ((((j & 1u) + e) ^ (g4 & 1u)) - e) * 32u + 8u * p;
  auto read_b = [&](int slot, int ks, bf16x8 (&bf)[5]) {
#pragma unroll
    for (int nt = 0; nt < 5; ++nt) { const unsigned ad = ba[nt & 1] + slot * BSZ + ks * 6144 + nt * 32; bf[nt] = cat(ldstr(ad), ldstr(ad + 3072)); }
  };
  f32x4 acc[2][5], accr[5];
#pragma unroll
  for (int nt = 0; nt < 5; ++nt) { acc[0][nt] = kZero4; acc[1][nt] = kZero4; accr[nt] = kZero4; }
  bf16x8 B1[2][5];
  {
#pragma unroll
    for (int ks = 0; ks < 2; ++ks) {
      bf16x8 bf[5];
      read_b(0, ks, bf);
      bf16x8 ar;
      if constexpr (KIND == 1) ar = lds128(ldsb + QREM + (2u * kh + ks) * 1024u + lane * 16u);
#pragma unroll
      for (int nt = 0; nt < 5; ++nt) {
        acc[0][nt] = MFMA16(bf[nt], A0[0][ks], acc[0][nt]); acc[1][nt] = MFMA16(bf[nt], A0[1][ks], acc[1][nt]);
        if constexpr (KIND == 1) accr[nt] = MFMA16(bf[nt], ar, accr[nt]);
      }
    }
    read_b(1, 0, B1[0]); read_b(1, 1, B1[1]);
  }
  bar();
  dma_b(a, 6u, w); dma_b(a, 7u, w);
  if (w == 0) {
    gu64* f = flags + (b * 32u + g * 8u + (lane & 7u)) * 16u;
    for (unsigned spin = 0; spin < (1u << 20); ++spin) {
      if (__all(__hip_atomic_load(f, __ATOMIC_RELAXED, __HIP_MEMORY_SCOPE_AGENT) == tag)) break;
      __builtin_amdgcn_s_sleep(1);
    }
  }
  STAMP(9);
  waitvm<0>();
  bar();
  STAMP(10);
  bf16x8 Af[8][2][2], Ar[8][2];
  auto load_a = [&](int s) {
    const unsigned so = ((g * 8u + ((j + s) & 7u)) * 4u + 2u * kh) * 1024u;
#pragma unroll
    for (int ks = 0; ks < 2; ++ks) {
#pragma unroll
      for (int qt = 0; qt < 2; ++qt) Af[s][qt][ks] = __builtin_bit_cast(bf16x8, __builtin_amdgcn_raw_buffer_load_b128(srdO, lane * 16u + ks * 1024u, (2u * w4 + (qt ^ kh)) * 131072u + so, LD_AUX));
      if (KIND != 0 && (s & 1) == (KIND - 1)) Ar[s][ks] = __builtin_bit_cast(bf16x8, __builtin_amdgcn_raw_buffer_load_b128(srdO, lane * 16u + ks * 1024u, 8u * 131072u + so, LD_AUX));
    }
  };
  load_a(1); load_a(2);
  GAP();
#pragma unroll
  for (int s = 1; s < 8; ++s) {
    bf16x8 Bn[2][5];
    if (s < 7) { read_b((s + 1) % 6, 0, Bn[0]); read_b((s + 1) % 6, 1, Bn[1]); }
#pragma unroll
    for (int ks = 0; ks < 2; ++ks)
#pragma unroll
      for (int nt = 0; nt < 5; ++nt) {
        acc[0][nt] = MFMA16(B1[ks][nt], Af[s][0][ks], acc[0][nt]); acc[1][nt] = MFMA16(B1[ks][nt], Af[s][1][ks], acc[1][nt]);
        if (KIND != 0 && (s & 1) == (KIND - 1)) accr[nt] = MFMA16(B1[ks][nt], Ar[s][ks], accr[nt]);
      }
#pragma unroll
    for (int i = 0; i < 20; ++i) { __builtin_amdgcn_sched_group_barrier(0x008, 1, 0); __builtin_amdgcn_sched_group_barrier(0x100, 1, 0); }
    GAP();
    if (s + 2 < 8) load_a(s + 2);
    GAP();
    if (s < 7) {
#pragma unroll
      for (int ks = 0; ks < 2; ++ks)
#pragma unroll
        for (int nt = 0; nt < 5; ++nt) B1[ks][nt] = Bn[ks][nt];
    }
  }
  STAMP(11);
  bar();
#pragma unroll
  for (int nt = 0; nt < 5; ++nt) *(LAS f32x4*)(ldsb + w * 5120u + nt * 1024u + lane * 16u) = acc[1][nt];
  if constexpr (KIND != 0) {
#pragma unroll
    for (int nt = 0; nt < 5; ++nt) *(LAS f32x4*)(ldsb + 40960u + w4 * 5120u + nt * 1024u + lane * 16u) = accr[nt];
  }
  bar();
  const __amdgpu_buffer_rsrc_t srdM = __builtin_amdgcn_make_buffer_rsrc((void*)(ws + WS_MBOX + (size_t)b * 32u * MBOX), (short)0, 32 * MBOX, 0x00020000);
  {
    const unsigned vo = q4 * (8u * (unsigned)MBOX) + (g4 * 4u + p) * 16u, so = j * (unsigned)MBOX + g * 11520u;
#pragma unroll
    for (int nt = 0; nt < 5; ++nt) {
      const f32x4 v = acc[0][nt] + *(LAS f32x4*)(ldsb + (w ^ 4u) * 5120u + nt * 1024u + lane * 16u);
      __builtin_amdgcn_raw_buffer_store_b128(__builtin_bit_cast(u32x4, v), srdM, vo, so + ((2u * w4 + kh) * 5u + nt) * 256u, ST_AUX);
    }
    if constexpr (KIND == 0) {
      for (unsigned nt = w4; nt < 5u; nt += 4u) {
        f32x4 v = kZero4;
#pragma unroll
        for (int i = 0; i < 4; ++i) v += *(LAS f32x4*)(ldsb + 40960u + i * 5120u + nt * 1024u + lane * 16u);
        __builtin_amdgcn_raw_buffer_store_b128(__builtin_bit_cast(u32x4, v), srdM, vo, so + (40u + nt) * 256u, ST_AUX);
      }
    }
  }
  STAMP(12);
  waitvm<0>();
  bar();
  if (w == 0) {
    if (lane == 0) __hip_atomic_store(flags + (256u + b * 32u + h) * 16u, tag, __ATOMIC_RELAXED, __HIP_MEMORY_SCOPE_AGENT);
    STAMP(13);
    gu64* f = flags + (256u + b * 32u + (lane & 3u) * 8u + j) * 16u;
    for (unsigned spin = 0; spin < (1u << 20); ++spin) {
      if (__all(__hip_atomic_load(f, __ATOMIC_RELAXED, __HIP_MEMORY_SCOPE_AGENT) == tag)) break;
      __builtin_amdgcn_s_sleep(1);
    }
    if (h == 0 && lane == 0) __hip_atomic_store(flags + (512u + b) * 16u, tag, __ATOMIC_RELAXED, __HIP_MEMORY_SCOPE_AGENT);
  }
  bar();
  STAMP(14);
#pragma unroll
  for (int k = 0; k < 2; ++k) {
    const unsigned u = w * 64u + lane + 512u * k;
    if (k == 0 || w < 4u) {
      f32x4 v[4];
#pragma unroll
      for (int i = 0; i < 4; ++i) v[i] = __builtin_bit_cast(f32x4, __builtin_amdgcn_raw_buffer_load_b128(srdM, u * 16u, h * (unsigned)MBOX + i * 11520u, LD_AUX));
      const f32x4 r = (v[0] + v[1]) + (v[2] + v[3]);
      const unsigned t = u >> 4, mt = t / 5u, nt = t - 5u * mt;
      if (u < 720u) *(u32x2*)(out + (size_t)(b * SQ + 16u * mt + 4u * g + (u & 3u)) * DM + 80u * j + 16u * nt + ((u >> 2) & 3u) * 4u) = u32x2{cvtpk(r[0], r[1]), cvtpk(r[2], r[3])};
    }
  }
  STAMP_END;
}

__global__ __launch_bounds__(512, 2) void attn_kernel(const u16* __restrict__ x, const u16* __restrict__ kc, const u16* __restrict__ vc,
                                                      const u16* __restrict__ wq, const u16* __restrict__ wo, char* ws, u16* __restrict__ out, unsigned long long* __restrict__ stamps) {
  extern __shared__ __attribute__((aligned(16))) char lds[];
  const unsigned ldsb = (unsigned)(size_t)(LAS char*)lds;
  const unsigned lane = threadIdx.x & 63u;
  const unsigned w = rfl(threadIdx.x >> 6);
  const unsigned bid = blockIdx.x, xg = bid & 7u, wi = bid >> 3;
  const unsigned h = (xg & 3u) * 8u + (wi & 7u), b = (xg >> 2) * 4u + (wi >> 3);
  STAMP_DECL;
  STAMP(0);
  if (w == 1) {
    unsigned long long pc; asm volatile("s_getpc_b64 %0" : "=s"(pc));
    const __amdgpu_buffer_rsrc_t srdC = __builtin_amdgcn_make_buffer_rsrc((void*)(pc & ~255ull), (short)0, CODE_LINES * 128, 0x00020000);
    unsigned ln = wi * ((CODE_LINES + 31) / 32) + lane; ln = ln >= CODE_LINES ? ln - CODE_LINES : ln;
    unsigned dummy;
    asm volatile("s_nop 4\n\tbuffer_load_dword %0, %1, %2, 0 offen\n\ts_waitcnt vmcnt(0)" : "=&v"(dummy) : "v"(ln * 128u), "s"(srdC) : "memory");
  }
  Att a;
  a.srdK = __builtin_amdgcn_make_buffer_rsrc((void*)(kc + (size_t)(b * NH + h) * SK * DH), (short)0, SK * DH * 2, 0x00020000);
  a.srdV = __builtin_amdgcn_make_buffer_rsrc((void*)(vc + (size_t)(b * NH + h) * SK * DH), (short)0, SK * DH * 2, 0x00020000);
  const unsigned w4 = w & 3u, kh = w >> 2;
  const bool loader = ((w + 2u) & 4u) != 0u;
  const unsigned r16 = lane & 15u, g4 = lane >> 4, q4 = r16 >> 2, p = lane & 3u;
  const unsigned prow = 4u * w4 + g4;
  a.koff = prow * 256u + ((r16 ^ prow) << 4);
  a.voff = prow * 256u + ((r16 ^ ((g4 << 2) | ((w4 & 1u) << 1))) << 4);
  a.rot = ((bid * 5u + (bid >> 4)) & 15u) * SLOT;
  a.m0k = ldsb + KR + w4 * 1024u; a.m0v = ldsb + VR + w4 * 1024u;
  auto dma_k = [&](unsigned t) { _Pragma("unroll") for (int j = 0; j < 4; ++j) dma_nt(a.m0k + (t & 3u) * SLOT + j * 4096u, a.koff, a.srdK, TOFF(t * SLOT) + j * 4096u); };
  auto dma_v = [&](unsigned t) { _Pragma("unroll") for (int j = 0; j < 4; ++j) dma_nt(a.m0v + (t & 3u) * SLOT + j * 4096u, a.voff, a.srdV, TOFF(t * SLOT) + j * 4096u); };
  const unsigned wsw = (q4 << 2) | ((g4 & 1u) << 1);
  unsigned long long tag = 0;
  if (w == 0) {
    const unsigned long long e = __hip_atomic_load((gu64*)(ws + WS_FLAG) + (512u + b) * 16u, __ATOMIC_RELAXED, __HIP_MEMORY_SCOPE_AGENT) + 1ull;
    tag = ((unsigned long long)rfl((unsigned)(e >> 32)) << 32) | rfl((unsigned)e);
  }

  if (!loader) {
    const unsigned qh = w4 & 1u, dh = w4 >> 1;
    f32x4 acc[4][4];
#pragma unroll
    for (int dt = 0; dt < 4; ++dt)
#pragma unroll
      for (int qt = 0; qt < 4; ++qt) acc[dt][qt] = kZero4;
    unsigned xa[2], wa[4];
#pragma unroll
    for (int s = 0; s < 2; ++s) xa[s] = ldsb + XS0 + (64u * qh + r16) * 128u + (((4u * s + g4) ^ (r16 >> 1)) << 4);
#pragma unroll
    for (int dt = 0; dt < 4; ++dt) wa[dt] = ldsb + WBASE + (8u * g4 + q4) * 256u + (((8u * dh + 2u * dt + (p >> 1)) ^ wsw) << 4) + 8u * (p & 1u);
    bf16x8 xf[2][4], wf[2][4];
#pragma unroll
    for (int n = 0; n <= 20; ++n) {
      const int sg = n >> 1, s = n & 1;
      if (n < 20 && s == 0) { bar(); GAP(); }
#pragma unroll
      for (int m = 0; m < 16; ++m) {
        if (n > 0) acc[m >> 2][m & 3] = MFMA16(wf[s ^ 1][m >> 2], xf[s ^ 1][m & 3], acc[m >> 2][m & 3]);
        if (n < 20 && m < 8) {
          const int j = m >> 1;
          if (m & 1) xf[s][j] = lds128(xa[s] + (sg % 3) * XSZ + j * 2048);
          else { const unsigned ad = wa[j] + wslot(sg) + s * 8192; wf[s][j] = cat(ldstr(ad), ldstr(ad + 1024)); }
        }
        GAP();
      }
    }
#pragma unroll
    for (int dt = 0; dt < 4; ++dt)
#pragma unroll
      for (int qt = 0; qt < 4; ++qt) {
        u32x2 q = {cvtpk(acc[dt][qt][0] * QSCALE, acc[dt][qt][1] * QSCALE), cvtpk(acc[dt][qt][2] * QSCALE, acc[dt][qt][3] * QSCALE)};
        *(LAS u32x2*)(ldsb + QI + (4u * qh + qt) * 4096u + (2u * dh + (dt >> 1)) * 1024u + ((2u * (dt & 1) + (g4 >> 1)) * 16u + r16) * 16u + (g4 & 1u) * 8u) = q;
      }
    STAMP(1);
    bar();
  } else {
    const __amdgpu_buffer_rsrc_t srdX = __builtin_amdgcn_make_buffer_rsrc((void*)(x + (size_t)b * SQ * DM), (short)0, SQ * DM * 2, 0x00020000);
    const __amdgpu_buffer_rsrc_t srdW = __builtin_amdgcn_make_buffer_rsrc((void*)(wq + h * DH), (short)0, (DM * HD - h * DH) * 2, 0x00020000);
    const unsigned xoff = (8u * w4 + (lane >> 3)) * (DM * 2u) + (((lane & 7u) ^ (4u * (w4 & 1u) + g4)) << 4);
    const unsigned woff = prow * (HD * 2u) + ((r16 ^ ((g4 << 2) | (((w4 >> 1) & 1u) << 1))) << 4);
    const unsigned rot = (wi * 3u) % 10u;
    auto dma_stage = [&](int sg) {
      unsigned kk = sg + rot; kk = kk >= 10u ? kk - 10u : kk;
      const unsigned wsl = WBASE + wslot(sg), xsl = XS0 + (sg % 3) * XSZ;
#pragma unroll
      for (int i = 0; i < 4; ++i) {
        DMAXW(ldsb + wsl + (w4 + 4u * i) * 1024u, woff, srdW, (kk * 64u + 16u * i) * (HD * 2u));
        DMAXW(ldsb + xsl + (w4 + 4u * i) * 1024u, xoff, srdX, kk * 128u + 32u * i * (DM * 2u));
      }
      if (w4 < 2) DMAXW(ldsb + xsl + (w4 + 16u) * 1024u, xoff, srdX, kk * 128u + 128u * (DM * 2u));
    };
    dma_stage(0); dma_stage(1);
    f32x4 acc[2] = {kZero4, kZero4};
    unsigned xa[2], wa[2];
#pragma unroll
    for (int s = 0; s < 2; ++s) xa[s] = ldsb + XS0 + (128u + r16) * 128u + (((4u * s + g4) ^ (r16 >> 1)) << 4);
#pragma unroll
    for (int e = 0; e < 2; ++e) wa[e] = ldsb + WBASE + (8u * g4 + q4) * 256u + (((4u * w4 + 2u * e + (p >> 1)) ^ wsw) << 4) + 8u * (p & 1u);
#pragma unroll
    for (int sg = 0; sg < 10; ++sg) {
      if (sg < 9) { if (w4 < 2) waitvm<9>(); else waitvm<8>(); }
      else waitvm<4>();
      bar();
      bf16x8 xf[2], wf[2][2];
#pragma unroll
      for (int s = 0; s < 2; ++s) {
        xf[s] = lds128(xa[s] + (sg % 3) * XSZ);
#pragma unroll
        for (int e = 0; e < 2; ++e) { const unsigned ad = wa[e] + wslot(sg) + s * 8192; wf[s][e] = cat(ldstr(ad), ldstr(ad + 1024)); }
      }
      GAP();
      if (sg + 2 < 10) dma_stage(sg + 2);
      if (sg == 8) dma_k(0);
      if (sg == 9) dma_k(1);
      GAP();
#pragma unroll
      for (int s = 0; s < 2; ++s)
#pragma unroll
        for (int e = 0; e < 2; ++e) acc[e] = MFMA16(wf[s][e], xf[s], acc[e]);
    }
#pragma unroll
    for (int e = 0; e < 2; ++e) {
      u32x2 q = {cvtpk(acc[e][0] * QSCALE, acc[e][1] * QSCALE), cvtpk(acc[e][2] * QSCALE, acc[e][3] * QSCALE)};
      *(LAS u32x2*)(ldsb + QREM + w4 * 1024u + ((2u * e + (g4 >> 1)) * 16u + r16) * 16u + (g4 & 1u) * 8u) = q;
    }
    STAMP(1);
    waitvm<4>();
    bar();
    dma_v(0);
  }
  STAMP(2);
#pragma unroll
  for (int s = 0; s < 4; ++s) a.ka[s] = ldsb + KR + (32u * kh + r16) * 256u + (((4u * s + g4) ^ r16) << 4);
#pragma unroll
  for (int dt = 0; dt < 8; ++dt) a.va[dt] = ldsb + VR + (32u * kh + 4u * g4 + q4) * 256u + (((2u * ((dt + 4u * kh) & 7u) + (p >> 1)) ^ wsw) << 4) + 8u * (p & 1u);
  a.qra = ldsb + QREM + lane * 16u;
  a.ldsb = ldsb; a.j = h & 7u; a.w4 = w4;
  a.srdB = __builtin_amdgcn_make_buffer_rsrc((void*)((const char*)wo + (size_t)(h >> 3) * 1024u * DM * 2u + (h & 7u) * 160u - (h & 1u) * 32u), (short)0, 1023 * DM * 2 + 192, 0x00020000);
  if (loader) attention<0>(a, ldsb, lane, w, b, h, ws, out, tag STAMP_PASS);
  else if (w & 1u) attention<2>(a, ldsb, lane, w, b, h, ws, out, tag STAMP_PASS);
  else attention<1>(a, ldsb, lane, w, b, h, ws, out, tag STAMP_PASS);

}

extern "C" void kernel_launch(void* const* d_in, const int* in_sizes, int n_in, void* d_out, int out_size, void* d_ws, size_t ws_size, hipStream_t stream) {
  const u16* x = (const u16*)d_in[0]; const u16* k = (const u16*)d_in[1]; const u16* v = (const u16*)d_in[2];
  const u16* wq = (const u16*)d_in[3]; const u16* wo = (const u16*)d_in[4];
  unsigned long long* stamps = (unsigned long long*)((char*)d_ws + (32u << 20));
  (void)hipFuncSetAttribute((const void*)attn_kernel, hipFuncAttributeMaxDynamicSharedMemorySize, LDS_A);
  attn_kernel<<<256, 512, LDS_A, stream>>>(x, k, v, wq, wo, (char*)d_ws, (u16*)d_out, stamps);
}
```

```cpp
#include <hip/hip_runtime.h>
#include <hip/hip_bf16.h>
#include <utility>

typedef unsigned short u16;
typedef float f32x4 __attribute__((ext_vector_type(4)));
typedef float f32x2 __attribute__((ext_vector_type(2)));
typedef __bf16 bf16x8 __attribute__((ext_vector_type(8)));
typedef __bf16 bf16x4 __attribute__((ext_vector_type(4)));
typedef __bf16 bf16x2 __attribute__((ext_vector_type(2)));
typedef unsigned u32x4 __attribute__((ext_vector_type(4)));
typedef unsigned u32x2 __attribute__((ext_vector_type(2)));
typedef __attribute__((address_space(1))) unsigned long long gu64;
typedef __amdgpu_buffer_rsrc_t srd_t;
#define LAS __attribute__((address_space(3)))
#define DI __device__ __forceinline__
#define GAP() __builtin_amdgcn_sched_barrier(0)

constexpr int SQ = 144, DM = 640, NH = 32, SK = 1024, DH = 128;
constexpr int HD = NH * DH;
constexpr float QSCALE = 0.12751743082f;
constexpr unsigned CODE_LINES = 192;

#define STAMPS 0

constexpr unsigned SLOT = 16384;
constexpr unsigned KR = 0, VR = 49152;
constexpr unsigned WS0 = 32768;
constexpr unsigned XS0 = 98304, XSZ = 18432;
constexpr unsigned QI = XS0 + XSZ;
constexpr unsigned PB = 98304;
constexpr unsigned STATS = 159744;
constexpr unsigned BSZ = 24576;
constexpr unsigned LDS_A = 163840;
constexpr unsigned bslot(int s) { return (s == 0 || s == 6) ? 135168u : (s == 1 || s == 7) ? BSZ : s == 2 ? 0u : (s - 1) * BSZ; }
constexpr size_t WS_MBOX = 9437184, MBOX = 46080;
constexpr size_t WS_FLAG = WS_MBOX + 256 * MBOX;

DI unsigned rfl(unsigned x) { return __builtin_amdgcn_readfirstlane(x); }
DI unsigned cvtpk(float lo, float hi) { f32x2 v = {lo, hi}; bf16x2 b = __builtin_convertvector(v, bf16x2); return __builtin_bit_cast(unsigned, b); }
DI bf16x8 lds128(unsigned a) { return *(LAS bf16x8*)a; }
DI bf16x4 ldstr(unsigned a) { return __builtin_amdgcn_ds_read_tr16_b64_v4bf16((LAS bf16x4*)a); }
DI bf16x8 cat(bf16x4 a, bf16x4 b) { return __builtin_shufflevector(a, b, 0, 1, 2, 3, 4, 5, 6, 7); }
DI srd_t mksrd(const void* p, unsigned bytes) { return __builtin_amdgcn_make_buffer_rsrc((void*)p, (short)0, bytes, 0x00020000); }
template <int N> DI void waitvm() { asm volatile("s_waitcnt vmcnt(%0)" ::"i"(N) : "memory"); }
DI void bar() { asm volatile("s_waitcnt lgkmcnt(0)\n\ts_barrier" ::: "memory"); GAP(); }
DI void dma(unsigned m0, unsigned voff, srd_t srd, unsigned soff) {
  asm volatile("s_mov_b32 m0, %0\n\ts_nop 0\n\tbuffer_load_dwordx4 %1, %2, %3 offen lds" ::"s"(m0), "v"(voff), "s"(srd), "s"(soff) : "memory");
}
DI void dma_nt(unsigned m0, unsigned voff, srd_t srd, unsigned soff) {
  asm volatile("s_mov_b32 m0, %0\n\ts_nop 0\n\tbuffer_load_dwordx4 %1, %2, %3 offen lds" ::"s"(m0), "v"(voff), "s"(srd), "s"(soff) : "memory");
}
DI void set_m0(unsigned m0) { asm volatile("s_mov_b32 m0, %0\n\ts_nop 0" ::"s"(m0) : "memory"); }
template <int OFF, bool NT> DI void dma_o(unsigned voff, srd_t srd, unsigned soff) {
  if constexpr (NT) asm volatile("buffer_load_dwordx4 %0, %1, %2 offen offset:%3 lds" ::"v"(voff), "s"(srd), "s"(soff), "i"(OFF) : "memory");
  else asm volatile("buffer_load_dwordx4 %0, %1, %2 offen offset:%3 lds" ::"v"(voff), "s"(srd), "s"(soff), "i"(OFF) : "memory");
}
template <bool NT> DI void dma4(unsigned m0, const unsigned (&voff)[4], srd_t srd, unsigned soff) {
  set_m0(m0); dma_o<0, NT>(voff[0], srd, soff); dma_o<1024, NT>(voff[1], srd, soff); dma_o<2048, NT>(voff[2], srd, soff); dma_o<3072, NT>(voff[3], srd, soff);
}
DI void dma_half(unsigned m0, unsigned voff, srd_t srd, unsigned soff) {
  asm volatile("s_mov_b32 m0, %0\n\ts_mov_b32 exec_hi, 0\n\tbuffer_load_dwordx4 %1, %2, %3 offen lds\n\ts_mov_b32 exec_hi, -1" ::"s"(m0), "v"(voff), "s"(srd), "s"(soff) : "memory");
}
DI void store_wt(u32x4 v, srd_t srd, unsigned voff, unsigned soff) { __builtin_amdgcn_raw_buffer_store_b128(v, srd, voff, soff, 16); GAP(); asm volatile("s_nop 1"); GAP(); }
DI float swap32_add(float x) { auto r = __builtin_amdgcn_permlane32_swap(__float_as_uint(x), __float_as_uint(x), false, false); return __uint_as_float(r[0]) + __uint_as_float(r[1]); }
DI float swap16_add(float x) { auto r = __builtin_amdgcn_permlane16_swap(__float_as_uint(x), __float_as_uint(x), false, false); return __uint_as_float(r[0]) + __uint_as_float(r[1]); }
DI float fadd(float a, float b) { float r; asm("v_add_f32_e32 %0, %1, %2" : "=v"(r) : "v"(a), "v"(b)); return r; }
DI unsigned fresh_lane() { unsigned l; asm volatile("v_mbcnt_lo_u32_b32 %0, -1, 0\n\tv_mbcnt_hi_u32_b32 %0, -1, %0" : "=v"(l)); return l; }
DI void mma(f32x4& d, bf16x8 a, bf16x8 b) { asm volatile("v_mfma_f32_16x16x32_bf16 %0, %1, %2, %0" : "+v"(d) : "v"(a), "v"(b)); GAP(); }
DI void mma0(f32x4& d, bf16x8 a, bf16x8 b) { asm volatile("v_mfma_f32_16x16x32_bf16 %0, %1, %2, 0" : "=&v"(d) : "v"(a), "v"(b)); GAP(); }
DI void mma_done() { GAP(); asm volatile("s_nop 7"); GAP(); }
const f32x4 kZero4 = {0.f, 0.f, 0.f, 0.f};
DI void zero(f32x4& d) { d = kZero4; asm volatile("" : "+v"(d)); }

#define STAMP(i) do {} while (0)
#define STAMP_END do {} while (0)

#define QSTAMP(i) do {} while (0)
#define TSTAMP(i) STAMP(i)
#define ROT 0u
struct Ctx {
  unsigned ldsb, w, w4, b, h, wi;
  unsigned long long tag;
  char* ws; u16* out; const u16* wo;
  srd_t srdK, srdV, srdB;
  unsigned koff[4], voff[4], boff[3];
};
DI void dma_k(const Ctx& c, unsigned t) { dma4<true>(c.ldsb + KR + (t % 3u) * SLOT + c.w4 * 4096u, c.koff, c.srdK, t * SLOT); }
DI void dma_v(const Ctx& c, unsigned t) { dma4<true>(c.ldsb + VR + ((t + 2u) % 3u) * SLOT + c.w4 * 4096u, c.voff, c.srdV, t * SLOT); }
DI void set_boff(Ctx& c, unsigned lane) {
#pragma unroll
  for (int i = 0; i < 3; ++i) { const unsigned qd = 16u * i + (lane >> 2), row = qd / 3u; c.boff[i] = row * (DM * 2u) + (qd - 3u * row) * 64u + (((lane & 3u) ^ (((row >> 2) & 1u) << 1)) << 4) - i * 1024u; }
}
DI void dma_b(const Ctx& c, int s, unsigned rb) {
  const unsigned m = c.ldsb + bslot(s) + rb * 3072u, so = ((((c.h + s) & 7u) << 7) + rb * 16u) * (DM * 2u);
  set_m0(m); dma_o<0, false>(c.boff[0], c.srdB, so); dma_o<1024, false>(c.boff[1], c.srdB, so); dma_o<2048, false>(c.boff[2], c.srdB, so);
}

template <int NQ> struct Att {
  f32x4 O[NQ][2], S[NQ];
  bf16x8 Q[NQ][4], K[4], V[2][2], P[NQ][2];
  u32x2 pk;
  float l[NQ];
  unsigned ka[4], va[2], pw, pr;
  unsigned rs, m0k, m0v;
  unsigned warm[3], wv;
};
template <int NQ> constexpr bool blk_pv(int b) { return b >= 2 * NQ - 3 ? true : (b >= 3 && (b & 1)); }
template <int NQ> constexpr int blk_tile(int b) { return b >= 2 * NQ - 3 ? b - NQ : b < 3 ? b : (b & 1) ? (b - 3) / 2 : (b + 2) / 2; }
template <int NQ> constexpr int pv_start(int t) { for (int b = 0; b < 2 * NQ; ++b) if (blk_pv<NQ>(b) && blk_tile<NQ>(b) == t) return 4 * b; return -1; }
template <int NQ, int T, int F, unsigned BUF> DI void sm_op(Att<NQ>& a) {
  if constexpr (F == 0 || F == 1) a.S[T][F] = __builtin_amdgcn_exp2f(a.S[T][F]);
  if constexpr (F == 3 || F == 4) a.S[T][F - 1] = __builtin_amdgcn_exp2f(a.S[T][F - 1]);
  if constexpr (F == 2) { a.l[T] = fadd(a.l[T], a.S[T][0]); GAP(); a.pk[0] = cvtpk(a.S[T][0], a.S[T][1]); }
  if constexpr (F == 5) { a.l[T] = fadd(a.l[T], a.S[T][1]); GAP(); a.pk[1] = cvtpk(a.S[T][2], a.S[T][3]); }
  if constexpr (F == 6) { *(LAS u32x2*)(a.pw + BUF + T * 2048) = a.pk; GAP(); a.l[T] = fadd(a.l[T], a.S[T][2]); GAP(); a.l[T] = fadd(a.l[T], a.S[T][3]); }
  GAP();
}
template <int NQ, int PAR, int MODE, int N> DI void gap(Ctx& c, Att<NQ>& a, unsigned i) {
  constexpr int G = 8 * NQ, B = N >> 2, X = N & 3, T = blk_tile<NQ>(B);
  constexpr unsigned CUR = PAR ? 0u : XSZ, NXT = PAR ? XSZ : 0u;
  if constexpr (blk_pv<NQ>(B)) mma(a.O[T][X & 1], a.V[X >> 1][X & 1], a.P[T][X >> 1]);
  else if constexpr (X == 0) mma0(a.S[T], a.K[0], a.Q[T][0]);
  else mma(a.S[T], a.K[X], a.Q[T][X]);
  if constexpr (N + G - 6 < 7 * NQ) sm_op<NQ, NQ - 1, N + G - 6 - 7 * (NQ - 1), CUR>(a);
  if constexpr (N == 2) {
    asm volatile("" : "+s"(a.rs));
    if constexpr (NQ == 4) { a.m0k = c.ldsb + KR + c.w4 * 4096u + a.rs; a.m0v = a.m0k + (VR - KR); }
    const unsigned d = a.rs == 2u * SLOT ? -2 * (int)SLOT : SLOT;
    a.rs += d;
#pragma unroll
    for (int s = 0; s < 4; ++s) a.ka[s] += d;
    a.va[0] += d; a.va[1] += d;
    GAP();
  }
  if constexpr (N == 3) {
    if constexpr (NQ == 4) waitvm<MODE <= 12 ? 8 : MODE == 13 ? 4 : 6>();
    bar();
  }
  if constexpr (N >= 3 && N <= 6) { constexpr int f = N - 3; const unsigned ad = a.va[f & 1] + (f >> 1) * 8192; a.V[f >> 1][f & 1] = cat(ldstr(ad), ldstr(ad + 4096)); GAP(); }
  if constexpr (N >= 6) sm_op<NQ, (N - 6) / 7, (N - 6) % 7, NXT>(a);
#pragma unroll
  for (int t = 0; t < NQ; ++t)
#pragma unroll
    for (int ks = 0; ks < 2; ++ks)
      if (N == pv_start<NQ>(t) - (t == 0 ? 5 : 6) + ks) { a.P[t][ks] = lds128(a.pr + CUR + t * 2048 + ks * 1024); GAP(); }
  if constexpr (B == 2 * NQ - 4 && MODE != 14) { a.K[X] = lds128(a.ka[X]); GAP(); }
  if constexpr (NQ == 4) {
    if constexpr (MODE <= 13 && N >= 5 && N <= 14 && (N - 5) % 3 == 0) { constexpr int j = (N - 5) / 3; if (j == 0) set_m0(a.m0v); dma_o<j * 1024, true>(c.voff[j], c.srdV, (i + 2u) * SLOT); GAP(); }
    if constexpr (MODE == 0 && N >= 18 && N <= 27 && (N - 18) % 3 == 0) { constexpr int j = (N - 18) / 3; if (j == 0) set_m0(a.m0k); dma_o<j * 1024, true>(c.koff[j], c.srdK, (i + 4u) * SLOT); GAP(); }
    if constexpr ((MODE == 13 && (N == 18 || N == 26)) || (MODE == 14 && N == 5)) {
      constexpr int k = MODE == 14 ? 2 : (N - 18) / 8;
      const srd_t s = mksrd((const char*)c.wo + ((size_t)(c.h >> 3) * 1024u + c.wi * 32u) * (DM * 2u), 32 * DM * 2);
      asm volatile("buffer_load_dword %0, %1, %2, %3 offen" : "=&v"(a.warm[k]) : "v"(a.wv), "s"(s), "s"(k * 16384u) : "memory");
      GAP();
    }
    if constexpr (MODE == 14 && (N == 16 || N == 26)) { dma_b(c, 0, 2u * c.w4 + (N > 16)); GAP(); }
  }
}
template <int NQ, int PAR, int MODE, int... Ns> DI void step_(Ctx& c, Att<NQ>& a, unsigned i, std::integer_sequence<int, Ns...>) { (gap<NQ, PAR, MODE, Ns>(c, a, i), ...); }
template <int NQ, int PAR, int MODE> DI void step(Ctx& c, Att<NQ>& a, unsigned i) { step_<NQ, PAR, MODE>(c, a, i, std::make_integer_sequence<int, 8 * NQ>{}); }
template <int NQ, unsigned BUF, int LO, int... Ns> DI void sm_run(Att<NQ>& a, std::integer_sequence<int, Ns...>) { ((sm_op<NQ, (LO + Ns) / 7, (LO + Ns) % 7, BUF>(a)), ...); }

template <int NQ> DI void attend_project(Ctx& c, unsigned lane) {
  constexpr bool L = NQ == 4;
  const unsigned ldsb = c.ldsb, w = c.w, w4 = c.w4, h = c.h, b = c.b;
  constexpr unsigned T0 = L ? 5u : 0u;
  const srd_t srdO = mksrd(c.ws + (size_t)b * 9u * 131072u, 9 * 131072);
  {
    Att<NQ> a;
    {
      const unsigned r16 = lane & 15u, g4 = lane >> 4, q4 = r16 >> 2, p = lane & 3u;
#pragma unroll
      for (int s = 0; s < 4; ++s) a.ka[s] = ldsb + KR + (16u * w4 + r16) * 256u + (((4u * s + g4) ^ r16) << 4);
#pragma unroll
      for (int e = 0; e < 2; ++e) a.va[e] = ldsb + VR + SLOT + (4u * g4 + q4) * 256u + (((4u * w4 + 2u * e + (p >> 1)) ^ ((q4 << 2) | ((g4 & 1u) << 1))) << 4) + 8u * (p & 1u);
      a.pr = ldsb + PB + T0 * 2048u + lane * 16u;
      a.pw = a.pr + (w4 >> 1) * 1024u + (w4 & 1u) * 8u;
    }
#pragma unroll
    for (int s = 0; s < 4; ++s) a.K[s] = lds128(a.ka[s]);
#pragma unroll
    for (int t = 0; t < NQ; ++t)
#pragma unroll
      for (int s = 0; s < 4; ++s) a.Q[t][s] = lds128(ldsb + QI + (T0 + t) * 4096u + s * 1024u + lane * 16u);
    if constexpr (L) waitvm<4>();
    bar();
    if constexpr (L) { dma_k(c, 2); GAP(); }
#pragma unroll
    for (int t = 0; t < NQ; ++t) {
      mma0(a.S[t], a.K[0], a.Q[t][0]);
#pragma unroll
      for (int s = 1; s < 4; ++s) mma(a.S[t], a.K[s], a.Q[t][s]);
    }
#pragma unroll
    for (int s = 0; s < 4; ++s) { a.ka[s] += SLOT; a.K[s] = lds128(a.ka[s]); }
    a.rs = SLOT;
#pragma unroll
    for (int t = 0; t < NQ; ++t) { zero(a.O[t][0]); zero(a.O[t][1]); a.l[t] = 0.f; }
    GAP();
    sm_run<NQ, XSZ, 0>(a, std::make_integer_sequence<int, 8 * NQ - 6>{});
    if constexpr (L) { dma_v(c, 1); dma_k(c, 3); GAP(); }
    STAMP(3);
#pragma nounroll
    for (unsigned i = 0; i < (L ? 12u : 14u); i += 2u) {
      step<NQ, 0, 0>(c, a, i);
      step<NQ, 1, 0>(c, a, i + 1u);
    }
    if constexpr (L) {
      { const unsigned l = fresh_lane(); set_boff(c, l); a.wv = (w4 * 64u + l) * 64u; GAP(); }
      step<NQ, 0, 12>(c, a, 12u);
      step<NQ, 1, 13>(c, a, 13u);
    }
    step<NQ, 0, 14>(c, a, 14u);
    asm volatile("s_nop 0");
    sm_run<NQ, 0u, 8 * NQ - 6>(a, std::make_integer_sequence<int, 6 - NQ>{});
    lane = fresh_lane();
    {
      const unsigned sa = ldsb + STATS + T0 * 256u + (lane & 15u) * 16u;
#pragma unroll
      for (int t = 0; t < NQ; ++t) {
        const float l = swap32_add(swap16_add(a.l[t]));
        if (lane < 16u) *(LAS float*)(sa + t * 256 + w4 * 4u) = l;
      }
      if constexpr (L) waitvm<9>(); else set_boff(c, lane);
      bar();
      dma_b(c, 1, w); GAP();
#pragma unroll
      for (int f = 0; f < 4; ++f) { const unsigned ad = a.va[f & 1] + SLOT + (f >> 1) * 8192; a.V[f >> 1][f & 1] = cat(ldstr(ad), ldstr(ad + 4096)); }
      f32x4 ls[NQ];
#pragma unroll
      for (int t = 0; t < NQ; ++t) { a.P[t][0] = lds128(a.pr + t * 2048); a.P[t][1] = lds128(a.pr + t * 2048 + 1024); }
#pragma unroll
      for (int t = 0; t < NQ; ++t) ls[t] = *(LAS f32x4*)(sa + t * 256);
      GAP();
#pragma unroll
      for (int t = 0; t < NQ; ++t)
#pragma unroll
        for (int x = 0; x < 4; ++x) mma(a.O[t][x & 1], a.V[x >> 1][x & 1], a.P[t][x >> 1]);
      mma_done();
      STAMP(6);
#pragma unroll
      for (int t = 0; t < NQ; ++t) {
        const float inv = __builtin_amdgcn_rcpf((ls[t][0] + ls[t][1]) + (ls[t][2] + ls[t][3]));
        const f32x4 o0 = a.O[t][0] * inv, o1 = a.O[t][1] * inv;
        const u32x4 f = {cvtpk(o0[0], o0[1]), cvtpk(o0[2], o0[3]), cvtpk(o1[0], o1[1]), cvtpk(o1[2], o1[3])};
        store_wt(f, srdO, lane * 16u, (T0 + t) * 131072u + (h * 4u + w4) * 1024u);
      }
      GAP();
      dma_b(c, 2, w); dma_b(c, 3, w);
      waitvm<6>();
      if constexpr (L) asm volatile("" ::"v"(a.warm[0]), "v"(a.warm[1]), "v"(a.warm[2]));
    }
  }
  TSTAMP(7);
  const unsigned kh = w >> 2, r16 = lane & 15u, g4 = lane >> 4, q4 = r16 >> 2, p = lane & 3u;
  const unsigned g = h >> 3, j = h & 7u;
  gu64* const flags = (gu64*)(c.ws + WS_FLAG);
  bar();
  if (!L && w == 0 && lane == 0) __hip_atomic_store(flags + (b * 32u + h) * 16u, c.tag, __ATOMIC_RELAXED, __HIP_MEMORY_SCOPE_AGENT);
  TSTAMP(8);
  unsigned ba[2][2], ao[2];
#pragma unroll
  for (int ks = 0; ks < 2; ++ks) {
    const unsigned ksa = ks ^ (w & 1u);
    ao[ks] = lane * 16u + (2u * kh + ksa) * 1024u;
#pragma unroll
    for (int e = 0; e < 2; ++e) ba[ks][e] = ldsb + (64u * kh + 32u * ksa + 4u * g4 + q4) * 192u + ((((j & 1u) + e) ^ (g4 & 1u)) - e) * 32u + 8u * p;
  }
  bf16x8 Bf[2][2][5], Af[2][2][2], Ar[2];
  auto read_b = [&](int par, int ks, int nt, unsigned slot) { const unsigned ad = ba[ks][nt & 1] + slot + nt * 32; Bf[par][ks][nt] = cat(ldstr(ad), ldstr(ad + 3072)); };
  auto a_off = [&](unsigned s) { return s < 8u ? (g * 8u + ((j + s) & 7u)) * 4096u : 0x40000000u; };
  auto load_a = [&](int par, int ks, unsigned so) {
#pragma unroll
    for (int qt = 0; qt < 2; ++qt) Af[par][ks][qt] = __builtin_bit_cast(bf16x8, __builtin_amdgcn_raw_buffer_load_b128(srdO, ao[ks], (2u * w4 + (qt ^ kh)) * 131072u + so, 16));
    if (!L && ks == 0) Ar[par] = __builtin_bit_cast(bf16x8, __builtin_amdgcn_raw_buffer_load_b128(srdO, ao[0], 8u * 131072u + so, 16));
  };
  load_a(0, 0, a_off(0)); load_a(0, 1, a_off(0));
  GAP();
#pragma unroll
  for (int par = 0; par < 2; ++par)
#pragma unroll
    for (int ks = 0; ks < 2; ++ks)
#pragma unroll
      for (int nt = 0; nt < 5; ++nt) read_b(par, ks, nt, bslot(par));
  GAP();
  dma_b(c, 4, w); dma_b(c, 5, w);
  f32x4 acc[2][5], accr[5];
#pragma unroll
  for (int nt = 0; nt < 5; ++nt) { zero(acc[0][nt]); zero(acc[1][nt]); if (!L) zero(accr[nt]); }
  bar();
  dma_b(c, 6, w); dma_b(c, 7, w);
  if (!L && w == 0) {
    gu64* f = flags + (b * 32u + g * 8u + (lane & 7u)) * 16u;
#pragma nounroll
    for (unsigned spin = 0; spin < (1u << 16); ++spin) {
      if (__all(__hip_atomic_load(f, __ATOMIC_RELAXED, __HIP_MEMORY_SCOPE_AGENT) == c.tag)) break;
      __builtin_amdgcn_s_sleep(1);
    }
  }
  TSTAMP(9);
  waitvm<0>();
  bar();
  TSTAMP(10);
  load_a(1, 0, a_off(1)); load_a(1, 1, a_off(1));
  GAP();
#pragma nounroll
  for (unsigned s = 0; s < 8u; s += 2u) {
#pragma unroll
    for (int par = 0; par < 2; ++par) {
      const unsigned sn = s + par + 2u, so = a_off(sn);
      const unsigned slot = sn == 2u ? 0u : sn == 6u ? bslot(6) : sn >= 7u ? BSZ : (sn - 1u) * BSZ;
#pragma unroll
      for (int ks = 0; ks < 2; ++ks) {
#pragma unroll
        for (int nt = 0; nt < 5; ++nt) {
          mma(acc[0][nt], Bf[par][ks][nt], Af[par][ks][0]);
          mma(acc[1][nt], Bf[par][ks][nt], Af[par][ks][1]);
          if (!L && ks == 0) mma(accr[nt], Bf[par][ks][nt], Ar[par]);
          read_b(par, ks, nt, slot); GAP();
        }
        load_a(par, ks, so); GAP();
      }
    }
  }
  mma_done();
  TSTAMP(11);
  bar();
#pragma unroll
  for (int nt = 0; nt < 5; ++nt) *(LAS f32x4*)(ldsb + w * 5120u + nt * 1024u + lane * 16u) = acc[1][nt];
  if constexpr (!L) {
#pragma unroll
    for (int nt = 0; nt < 5; ++nt) *(LAS f32x4*)(ldsb + 40960u + w4 * 5120u + nt * 1024u + lane * 16u) = accr[nt];
  }
  bar();
  const srd_t srdM = mksrd(c.ws + WS_MBOX + (size_t)b * 32u * MBOX, 32 * MBOX);
  {
    const unsigned vo = q4 * (8u * (unsigned)MBOX) + (g4 * 4u + p) * 16u, so = j * (unsigned)MBOX + g * 11520u;
#pragma unroll
    for (int nt = 0; nt < 5; ++nt) {
      const f32x4 v = acc[0][nt] + *(LAS f32x4*)(ldsb + (w ^ 4u) * 5120u + nt * 1024u + lane * 16u);
      store_wt(__builtin_bit_cast(u32x4, v), srdM, vo, so + ((2u * w4 + kh) * 5u + nt) * 256u);
    }
    if constexpr (L) {
#pragma nounroll
      for (unsigned nt = w4; nt < 5u; nt += 4u) {
        f32x4 v = kZero4;
#pragma unroll
        for (int i = 0; i < 4; ++i) v += *(LAS f32x4*)(ldsb + 40960u + i * 5120u + nt * 1024u + lane * 16u);
        store_wt(__builtin_bit_cast(u32x4, v), srdM, vo, so + (40u + nt) * 256u);
      }
    }
  }
  TSTAMP(12);
  waitvm<0>();
  bar();
  if (!L && w == 0) {
    if (lane == 0) __hip_atomic_store(flags + (256u + b * 32u + h) * 16u, c.tag, __ATOMIC_RELAXED, __HIP_MEMORY_SCOPE_AGENT);
    TSTAMP(13);
    gu64* f = flags + (256u + b * 32u + (lane & 3u) * 8u + j) * 16u;
#pragma nounroll
    for (unsigned spin = 0; spin < (1u << 16); ++spin) {
      if (__all(__hip_atomic_load(f, __ATOMIC_RELAXED, __HIP_MEMORY_SCOPE_AGENT) == c.tag)) break;
      __builtin_amdgcn_s_sleep(1);
    }
    if (h == 0 && lane == 0) __hip_atomic_store(flags + (512u + b) * 16u, c.tag, __ATOMIC_RELAXED, __HIP_MEMORY_SCOPE_AGENT);
  }
  bar();
  TSTAMP(14);
  {
    f32x4 v[2][4];
#pragma unroll
    for (int k = 0; k < 2; ++k)
#pragma unroll
      for (int i = 0; i < 4; ++i) {
        const unsigned u = w * 64u + lane + 512u * k;
        v[k][i] = __builtin_bit_cast(f32x4, __builtin_amdgcn_raw_buffer_load_b128(srdM, u < 720u ? u * 16u : 0x40000000u, h * (unsigned)MBOX + i * 11520u, 16));
      }
#pragma unroll
    for (int k = 0; k < 2; ++k) {
      const unsigned u = w * 64u + lane + 512u * k;
      const f32x4 r = (v[k][0] + v[k][1]) + (v[k][2] + v[k][3]);
      const unsigned t = u >> 4, mt = t / 5u, nt = t - 5u * mt;
      if (u < 720u) *(u32x2*)(c.out + (size_t)(b * SQ + 16u * mt + 4u * g + (u & 3u)) * DM + 80u * j + 16u * nt + ((u >> 2) & 3u) * 4u) = u32x2{cvtpk(r[0], r[1]), cvtpk(r[2], r[3])};
    }
  }
  STAMP_END;
}

__global__ __launch_bounds__(512, 2) void fused_kernel(const u16* __restrict__ x, const u16* __restrict__ kc, const u16* __restrict__ vc,
                                                       const u16* __restrict__ wq, const u16* __restrict__ wo, char* ws, u16* __restrict__ out) {
  extern __shared__ __attribute__((aligned(16))) char lds[];
  unsigned long long pc; asm volatile("s_getpc_b64 %0" : "=s"(pc));
  Ctx c;
  STAMP(0);
  const unsigned ldsb = (unsigned)(size_t)(LAS char*)lds;
  const unsigned lane = threadIdx.x & 63u;
  const unsigned w = rfl(threadIdx.x >> 6);
  const unsigned bid = blockIdx.x, xg = bid & 7u, wi = bid >> 3;
  const unsigned h = (xg & 3u) * 8u + (wi & 7u), b = (xg >> 2) * 4u + (wi >> 3);
  const unsigned w4 = w & 3u;
  const bool loader = ((w + 2u) & 4u) != 0u;
  const unsigned r16 = lane & 15u, g4 = lane >> 4, q4 = r16 >> 2, p = lane & 3u;
  const unsigned wsw = (q4 << 2) | ((g4 & 1u) << 1);
  c.ldsb = ldsb; c.w = w; c.w4 = w4; c.b = b; c.h = h; c.wi = wi; c.ws = ws; c.out = out; c.wo = wo; c.tag = 0;
  c.srdB = mksrd((const char*)wo + (size_t)(h >> 3) * 1024u * DM * 2u + (h & 7u) * 160u - (h & 1u) * 32u, 1023 * DM * 2 + 192);

  if (loader) {
    const srd_t srdX = mksrd(x + (size_t)b * SQ * DM, SQ * DM * 2);
    const srd_t srdW = mksrd(wq + h * DH, (DM * HD - h * DH) * 2);
    unsigned xoff[4], woff[4];
#pragma unroll
    for (int i = 0; i < 4; ++i) {
      xoff[i] = (32u * w4 + 8u * i + (lane >> 3)) * (DM * 2u) + (((lane & 7u) ^ (4u * (i & 1) + g4)) << 4) - i * 1024u;
      woff[i] = (16u * w4 + 4u * i + g4) * (HD * 2u) + ((r16 ^ ((g4 << 2) | (i & 2))) << 4) - i * 1024u;
    }
    const unsigned xoff4 = (128u + 4u * w4 + (lane >> 3)) * (DM * 2u) + (((lane & 7u) ^ ((2u * w4 + g4) & 7u)) << 4);
    unsigned kk = ROT;
    auto dma_stage = [&](unsigned wsl, unsigned xsl) {
      dma4<false>(ldsb + WS0 + wsl + w4 * 4096u, woff, srdW, kk * (64u * HD * 2u));
      dma4<false>(ldsb + XS0 + xsl + w4 * 4096u, xoff, srdX, kk * 128u);
      dma_half(ldsb + XS0 + xsl + 16384u + w4 * 512u, xoff4, srdX, kk * 128u);
      kk = kk == 9u ? 0u : kk + 1u;
    };
    QSTAMP(7);
    dma_stage(2u * SLOT, 0u);
    QSTAMP(8);
    dma_stage(0u, XSZ);
    QSTAMP(9);
    c.srdK = mksrd(kc + (size_t)(b * NH + h) * SK * DH, SK * DH * 2);
    c.srdV = mksrd(vc + (size_t)(b * NH + h) * SK * DH, SK * DH * 2);
#pragma unroll
    for (int i = 0; i < 4; ++i) {
      c.koff[i] = (16u * w4 + g4) * 256u + ((r16 ^ (4u * i + g4)) << 4);
      c.voff[i] = (16u * w4 + g4) * 256u + ((r16 ^ ((g4 << 2) | ((i & 1) << 1))) << 4);
    }
    f32x4 acc[2] = {kZero4, kZero4};
    unsigned xa[2], wa[2];
#pragma unroll
    for (int s = 0; s < 2; ++s) xa[s] = ldsb + XS0 + (128u + r16) * 128u + (((4u * s + g4) ^ (r16 >> 1)) << 4);
#pragma unroll
    for (int e = 0; e < 2; ++e) wa[e] = ldsb + WS0 + (8u * g4 + q4) * 256u + (((4u * w4 + 2u * e + (p >> 1)) ^ wsw) << 4) + 8u * (p & 1u);
    bf16x8 xf[2], wf[2][2];
    xf[0] = xf[1] = wf[0][0] = wf[0][1] = wf[1][0] = wf[1][1] = bf16x8{};
    auto mm = [&]() {
#pragma unroll
      for (int s = 0; s < 2; ++s)
#pragma unroll
        for (int e = 0; e < 2; ++e) acc[e] = __builtin_amdgcn_mfma_f32_16x16x32_bf16(wf[s][e], xf[s], acc[e], 0, 0, 0);
      GAP();
    };
    auto stage = [&](unsigned wsl, unsigned xsl, unsigned wprev, unsigned xprev, int kind) {
      if (kind == 2) waitvm<4>(); else waitvm<9>();
      bar();
      mm();
#pragma unroll
      for (int s = 0; s < 2; ++s) {
        xf[s] = lds128(xa[s] + xsl);
#pragma unroll
        for (int e = 0; e < 2; ++e) { const unsigned ad = wa[e] + wsl + s * 8192; wf[s][e] = cat(ldstr(ad), ldstr(ad + 1024)); }
      }
      GAP();
      if (kind == 0) dma_stage(wprev, xprev); else dma_k(c, kind - 1);
      GAP();
    };
    __builtin_amdgcn_s_setprio(1);
    stage(2u * SLOT, 0u, SLOT, 2u * XSZ, 0);
    QSTAMP(12);
    stage(0u, XSZ, 2u * SLOT, 0u, 0);
#pragma nounroll
    for (unsigned it = 0; it < 2u; ++it) {
      stage(SLOT, 2u * XSZ, 0u, XSZ, 0);
      stage(2u * SLOT, 0u, SLOT, 2u * XSZ, 0);
      if (it == 0) QSTAMP(13); else QSTAMP(14);
      stage(0u, XSZ, 2u * SLOT, 0u, 0);
    }
    stage(SLOT, 2u * XSZ, 0u, 0u, 1);
    stage(2u * SLOT, 0u, 0u, 0u, 2);
    bar();
    mm();
    __builtin_amdgcn_s_setprio(0);
#pragma unroll
    for (int e = 0; e < 2; ++e) {
      u32x2 q = {cvtpk(acc[e][0] * QSCALE, acc[e][1] * QSCALE), cvtpk(acc[e][2] * QSCALE, acc[e][3] * QSCALE)};
      *(LAS u32x2*)(ldsb + QI + 8u * 4096u + w4 * 1024u + ((2u * e + (g4 >> 1)) * 16u + r16) * 16u + (g4 & 1u) * 8u) = q;
    }
    STAMP(1);
    waitvm<4>();
    bar();
    dma_v(c, 0);
    STAMP(2);
    attend_project<4>(c, lane);
  } else {
    if (w == 0) {
      const unsigned long long e = __hip_atomic_load((gu64*)(ws + WS_FLAG) + (512u + b) * 16u, __ATOMIC_RELAXED, __HIP_MEMORY_SCOPE_AGENT) + 1ull;
      c.tag = ((unsigned long long)rfl((unsigned)(e >> 32)) << 32) | rfl((unsigned)e);
    }
    if (w == 1) {
      const srd_t srdC = mksrd((const void*)(pc & ~255ull), CODE_LINES * 128);
      unsigned ln = wi * (CODE_LINES / 32) + (lane >> 1); ln = ln >= CODE_LINES ? ln - CODE_LINES : ln;
      unsigned dummy;
      asm volatile("s_nop 4\n\tbuffer_load_dword %0, %1, %2, 0 offen\n\ts_waitcnt vmcnt(0)" : "=&v"(dummy) : "v"(ln * 128u + (lane & 1u) * 64u), "s"(srdC) : "memory");
    }
    const unsigned qh = w4 & 1u, dh = w4 >> 1;
    f32x4 acc[4][4];
#pragma unroll
    for (int dt = 0; dt < 4; ++dt)
#pragma unroll
      for (int qt = 0; qt < 4; ++qt) zero(acc[dt][qt]);
    unsigned xa[2], wa[4];
#pragma unroll
    for (int s = 0; s < 2; ++s) xa[s] = ldsb + XS0 + (64u * qh + r16) * 128u + (((4u * s + g4) ^ (r16 >> 1)) << 4);
#pragma unroll
    for (int dt = 0; dt < 4; ++dt) wa[dt] = ldsb + WS0 + 2u * SLOT + (8u * g4 + q4) * 256u + (((8u * dh + 2u * dt + (p >> 1)) ^ wsw) << 4) + 8u * (p & 1u);
    bf16x8 xf[2][4];
    bf16x4 wl[2][4], wu[2][4];
    auto rd = [&](int s, int r) {
      if (r == 0) wl[s][0] = ldstr(wa[0] + s * 8192);
      else if (r == 1) wu[s][0] = ldstr(wa[0] + s * 8192 + 1024);
      else if (r < 6) xf[s][r - 2] = lds128(xa[s] + (r - 2) * 2048);
      else if (r & 1) wu[s][(r - 4) >> 1] = ldstr(wa[(r - 4) >> 1] + s * 8192 + 1024);
      else wl[s][(r - 4) >> 1] = ldstr(wa[(r - 4) >> 1] + s * 8192);
      GAP();
    };
    bar();
#pragma unroll
    for (int r = 0; r < 12; ++r) rd(0, r);
    unsigned sl = 0, dx = XSZ, dw = -2 * (int)SLOT;
#pragma nounroll
    for (unsigned sg = 0; sg < 10u; ++sg) {
#pragma unroll
      for (int m = 0; m < 16; ++m) {
        mma(acc[m >> 2][m & 3], cat(wl[0][m >> 2], wu[0][m >> 2]), xf[0][m & 3]);
        if (m < 12) rd(1, m);
        if (m == 12) { xa[0] += dx; xa[1] += dx; GAP(); }
        if (m == 13) { wa[0] += dw; wa[1] += dw; GAP(); }
        if (m == 14) { wa[2] += dw; wa[3] += dw; GAP(); }
      }
#pragma unroll
      for (int m = 0; m < 16; ++m) {
        mma(acc[m >> 2][m & 3], cat(wl[1][m >> 2], wu[1][m >> 2]), xf[1][m & 3]);
        if (m == 0) bar();
        if (m < 12) rd(0, m);
        if (m == 13) { sl = sl == 2u ? 0u : sl + 1u; asm volatile("" : "+s"(sl)); GAP(); }
        if (m == 14) { dx = sl == 2u ? -2 * (int)XSZ : XSZ; asm volatile("" : "+s"(dx)); GAP(); }
        if (m == 15) { dw = sl == 0u ? -2 * (int)SLOT : SLOT; asm volatile("" : "+s"(dw)); GAP(); }
      }
    }
    mma_done();
#pragma unroll
    for (int dt = 0; dt < 4; ++dt)
#pragma unroll
      for (int qt = 0; qt < 4; ++qt) {
        u32x2 q = {cvtpk(acc[dt][qt][0] * QSCALE, acc[dt][qt][1] * QSCALE), cvtpk(acc[dt][qt][2] * QSCALE, acc[dt][qt][3] * QSCALE)};
        *(LAS u32x2*)(ldsb + QI + (4u * qh + qt) * 4096u + (2u * dh + (dt >> 1)) * 1024u + ((2u * (dt & 1) + (g4 >> 1)) * 16u + r16) * 16u + (g4 & 1u) * 8u) = q;
      }
    STAMP(1);
    bar();
    STAMP(2);
    attend_project<5>(c, lane);
  }
}

extern "C" void kernel_launch(void* const* d_in, const int* in_sizes, int n_in, void* d_out, int out_size, void* d_ws, size_t ws_size, hipStream_t stream) {
  (void)hipFuncSetAttribute((const void*)fused_kernel, hipFuncAttributeMaxDynamicSharedMemorySize, LDS_A);
  fused_kernel<<<256, 512, LDS_A, stream>>>((const u16*)d_in[0], (const u16*)d_in[1], (const u16*)d_in[2], (const u16*)d_in[3], (const u16*)d_in[4], (char*)d_ws, (u16*)d_out);
}
```

```cpp
#include <hip/hip_runtime.h>
#include <hip/hip_bf16.h>
#include <utility>

typedef unsigned short u16;
typedef float f32x4 __attribute__((ext_vector_type(4)));
typedef float f32x2 __attribute__((ext_vector_type(2)));
typedef __bf16 bf16x8 __attribute__((ext_vector_type(8)));
typedef __bf16 bf16x4 __attribute__((ext_vector_type(4)));
typedef __bf16 bf16x2 __attribute__((ext_vector_type(2)));
typedef unsigned u32x4 __attribute__((ext_vector_type(4)));
typedef unsigned u32x2 __attribute__((ext_vector_type(2)));
typedef __attribute__((address_space(1))) unsigned long long gu64;
typedef __amdgpu_buffer_rsrc_t srd_t;
#define LAS __attribute__((address_space(3)))
#define DI __device__ __forceinline__
#define GAP() __builtin_amdgcn_sched_barrier(0)

constexpr int SQ = 144, DM = 640, NH = 32, SK = 1024, DH = 128;
constexpr int HD = NH * DH;
constexpr float QSCALE = 0.12751743082f;
constexpr unsigned CODE_LINES = 192;

#define STAMPS 1
#define PROBE_A 0
#define PROBE_B 1
#define PROBE_SEL 0

constexpr unsigned SLOT = 16384;
constexpr unsigned KR = 0, VR = 49152;
constexpr unsigned WS0 = 32768;
constexpr unsigned XS0 = 98304, XSZ = 18432;
constexpr unsigned QI = XS0 + XSZ;
constexpr unsigned PB = 98304;
constexpr unsigned STATS = 159744;
constexpr unsigned BSZ = 24576;
constexpr unsigned LDS_A = 163840;
constexpr unsigned bslot(int s) { return (s == 0 || s == 6) ? 135168u : (s == 1 || s == 7) ? BSZ : s == 2 ? 0u : (s - 1) * BSZ; }
constexpr size_t WS_MBOX = 9437184, MBOX = 46080;
constexpr size_t WS_FLAG = WS_MBOX + 256 * MBOX;

DI unsigned rfl(unsigned x) { return __builtin_amdgcn_readfirstlane(x); }
DI unsigned cvtpk(float lo, float hi) { f32x2 v = {lo, hi}; bf16x2 b = __builtin_convertvector(v, bf16x2); return __builtin_bit_cast(unsigned, b); }
DI bf16x8 lds128(unsigned a) { return *(LAS bf16x8*)a; }
DI bf16x4 ldstr(unsigned a) { return __builtin_amdgcn_ds_read_tr16_b64_v4bf16((LAS bf16x4*)a); }
DI bf16x8 cat(bf16x4 a, bf16x4 b) { return __builtin_shufflevector(a, b, 0, 1, 2, 3, 4, 5, 6, 7); }
DI srd_t mksrd(const void* p, unsigned bytes) { return __builtin_amdgcn_make_buffer_rsrc((void*)p, (short)0, bytes, 0x00020000); }
template <int N> DI void waitvm() { asm volatile("s_waitcnt vmcnt(%0)" ::"i"(N) : "memory"); }
DI void bar() { asm volatile("s_waitcnt lgkmcnt(0)\n\ts_barrier" ::: "memory"); GAP(); }
DI void dma(unsigned m0, unsigned voff, srd_t srd, unsigned soff) {
  asm volatile("s_mov_b32 m0, %0\n\ts_nop 0\n\tbuffer_load_dwordx4 %1, %2, %3 offen lds" ::"s"(m0), "v"(voff), "s"(srd), "s"(soff) : "memory");
}
DI void dma_nt(unsigned m0, unsigned voff, srd_t srd, unsigned soff) {
  asm volatile("s_mov_b32 m0, %0\n\ts_nop 0\n\tbuffer_load_dwordx4 %1, %2, %3 offen nt lds" ::"s"(m0), "v"(voff), "s"(srd), "s"(soff) : "memory");
}
DI void set_m0(unsigned m0) { asm volatile("s_mov_b32 m0, %0\n\ts_nop 0" ::"s"(m0) : "memory"); }
template <int OFF, bool NT> DI void dma_o(unsigned voff, srd_t srd, unsigned soff) {
  if constexpr (NT) asm volatile("buffer_load_dwordx4 %0, %1, %2 offen offset:%3 nt lds" ::"v"(voff), "s"(srd), "s"(soff), "i"(OFF) : "memory");
  else asm volatile("buffer_load_dwordx4 %0, %1, %2 offen offset:%3 lds" ::"v"(voff), "s"(srd), "s"(soff), "i"(OFF) : "memory");
}
template <bool NT> DI void dma4(unsigned m0, const unsigned (&voff)[4], srd_t srd, unsigned soff) {
  set_m0(m0); dma_o<0, NT>(voff[0], srd, soff); dma_o<1024, NT>(voff[1], srd, soff); dma_o<2048, NT>(voff[2], srd, soff); dma_o<3072, NT>(voff[3], srd, soff);
}
DI void dma_half(unsigned m0, unsigned voff, srd_t srd, unsigned soff) {
  asm volatile("s_mov_b32 m0, %0\n\ts_mov_b32 exec_hi, 0\n\tbuffer_load_dwordx4 %1, %2, %3 offen lds\n\ts_mov_b32 exec_hi, -1" ::"s"(m0), "v"(voff), "s"(srd), "s"(soff) : "memory");
}
DI void store_wt(u32x4 v, srd_t srd, unsigned voff, unsigned soff) { __builtin_amdgcn_raw_buffer_store_b128(v, srd, voff, soff, 16); GAP(); asm volatile("s_nop 1"); GAP(); }
DI float swap32_add(float x) { auto r = __builtin_amdgcn_permlane32_swap(__float_as_uint(x), __float_as_uint(x), false, false); return __uint_as_float(r[0]) + __uint_as_float(r[1]); }
DI float swap16_add(float x) { auto r = __builtin_amdgcn_permlane16_swap(__float_as_uint(x), __float_as_uint(x), false, false); return __uint_as_float(r[0]) + __uint_as_float(r[1]); }
DI float fadd(float a, float b) { float r; asm("v_add_f32_e32 %0, %1, %2" : "=v"(r) : "v"(a), "v"(b)); return r; }
DI unsigned fresh_lane() { unsigned l; asm volatile("v_mbcnt_lo_u32_b32 %0, -1, 0\n\tv_mbcnt_hi_u32_b32 %0, -1, %0" : "=v"(l)); return l; }
DI void mma(f32x4& d, bf16x8 a, bf16x8 b) { asm volatile("v_mfma_f32_16x16x32_bf16 %0, %1, %2, %0" : "+v"(d) : "v"(a), "v"(b)); GAP(); }
DI void mma0(f32x4& d, bf16x8 a, bf16x8 b) { asm volatile("v_mfma_f32_16x16x32_bf16 %0, %1, %2, 0" : "=&v"(d) : "v"(a), "v"(b)); GAP(); }
DI void mma_done() { GAP(); asm volatile("s_nop 7"); GAP(); }
const f32x4 kZero4 = {0.f, 0.f, 0.f, 0.f};
DI void zero(f32x4& d) { d = kZero4; asm volatile("" : "+v"(d)); }

struct Stamps { unsigned t[16], c[16]; };
#define STAMP(i) do { unsigned long long a_, b_; asm volatile("s_memrealtime %0\n\ts_memtime %1\n\ts_waitcnt lgkmcnt(0)" : "=s"(a_), "=s"(b_) :: "memory"); c.st.t[i] = (unsigned)a_ | 1u; c.st.c[i] = (unsigned)b_; } while (0)
#define STAMP_END do { waitvm<0>(); STAMP(15); if (c.w == 0 && (PROBE_SEL)) { const unsigned lim = 10000u; \
  for (unsigned n_ = 0; n_ < 1000000u; ++n_) { unsigned long long a_; asm volatile("s_memrealtime %0\n\ts_waitcnt lgkmcnt(0)" : "=s"(a_) :: "memory"); if ((unsigned)a_ - c.st.t[0] >= lim) break; __builtin_amdgcn_s_sleep(2); } } } while (0)

#define QSTAMP(i) do {} while (0)
#define TSTAMP(i) STAMP(i)
#define ROT 0u
struct Ctx {
  unsigned ldsb, w, w4, b, h, wi;
  unsigned long long tag;
  char* ws; u16* out; const u16* wo;
  srd_t srdK, srdV, srdB;
  unsigned koff[4], voff[4], boff[3];
  Stamps st;
};
DI void dma_k(const Ctx& c, unsigned t) { dma4<true>(c.ldsb + KR + (t % 3u) * SLOT + c.w4 * 4096u, c.koff, c.srdK, t * SLOT); }
DI void dma_v(const Ctx& c, unsigned t) { dma4<true>(c.ldsb + VR + ((t + 2u) % 3u) * SLOT + c.w4 * 4096u, c.voff, c.srdV, t * SLOT); }
DI void set_boff(Ctx& c, unsigned lane) {
#pragma unroll
  for (int i = 0; i < 3; ++i) { const unsigned qd = 16u * i + (lane >> 2), row = qd / 3u; c.boff[i] = row * (DM * 2u) + (qd - 3u * row) * 64u + (((lane & 3u) ^ (((row >> 2) & 1u) << 1)) << 4) - i * 1024u; }
}
DI void dma_b(const Ctx& c, int s, unsigned rb) {
  const unsigned m = c.ldsb + bslot(s) + rb * 3072u, so = ((((c.h + s) & 7u) << 7) + rb * 16u) * (DM * 2u);
  set_m0(m); dma_o<0, false>(c.boff[0], c.srdB, so); dma_o<1024, false>(c.boff[1], c.srdB, so); dma_o<2048, false>(c.boff[2], c.srdB, so);
}

template <int NQ> struct Att {
  f32x4 O[NQ][2], S[NQ];
  bf16x8 Q[NQ][4], K[4], V[2][2], P[NQ][2];
  u32x2 pk;
  float l[NQ];
  unsigned ka[4], va[2], pw, pr;
  unsigned rs, m0k, m0v;
  unsigned warm[3], wv;
};
template <int NQ> constexpr bool blk_pv(int b) { return b >= 2 * NQ - 3 ? true : (b >= 3 && (b & 1)); }
template <int NQ> constexpr int blk_tile(int b) { return b >= 2 * NQ - 3 ? b - NQ : b < 3 ? b : (b & 1) ? (b - 3) / 2 : (b + 2) / 2; }
template <int NQ> constexpr int pv_start(int t) { for (int b = 0; b < 2 * NQ; ++b) if (blk_pv<NQ>(b) && blk_tile<NQ>(b) == t) return 4 * b; return -1; }
template <int NQ, int T, int F, unsigned BUF> DI void sm_op(Att<NQ>& a) {
  if constexpr (F == 0 || F == 1) a.S[T][F] = __builtin_amdgcn_exp2f(a.S[T][F]);
  if constexpr (F == 3 || F == 4) a.S[T][F - 1] = __builtin_amdgcn_exp2f(a.S[T][F - 1]);
  if constexpr (F == 2) { a.l[T] = fadd(a.l[T], a.S[T][0]); GAP(); a.pk[0] = cvtpk(a.S[T][0], a.S[T][1]); }
  if constexpr (F == 5) { a.l[T] = fadd(a.l[T], a.S[T][1]); GAP(); a.pk[1] = cvtpk(a.S[T][2], a.S[T][3]); }
  if constexpr (F == 6) { *(LAS u32x2*)(a.pw + BUF + T * 2048) = a.pk; GAP(); a.l[T] = fadd(a.l[T], a.S[T][2]); GAP(); a.l[T] = fadd(a.l[T], a.S[T][3]); }
  GAP();
}
template <int NQ, int PAR, int MODE, int N> DI void gap(Ctx& c, Att<NQ>& a, unsigned i) {
  constexpr int G = 8 * NQ, B = N >> 2, X = N & 3, T = blk_tile<NQ>(B);
  constexpr unsigned CUR = PAR ? 0u : XSZ, NXT = PAR ? XSZ : 0u;
  if constexpr (blk_pv<NQ>(B)) mma(a.O[T][X & 1], a.V[X >> 1][X & 1], a.P[T][X >> 1]);
  else if constexpr (X == 0) mma0(a.S[T], a.K[0], a.Q[T][0]);
  else mma(a.S[T], a.K[X], a.Q[T][X]);
  if constexpr (N + G - 6 < 7 * NQ) sm_op<NQ, NQ - 1, N + G - 6 - 7 * (NQ - 1), CUR>(a);
  if constexpr (N == 2) {
    asm volatile("" : "+s"(a.rs));
    if constexpr (NQ == 4) { a.m0k = c.ldsb + KR + c.w4 * 4096u + a.rs; a.m0v = a.m0k + (VR - KR); }
    const unsigned d = a.rs == 2u * SLOT ? -2 * (int)SLOT : SLOT;
    a.rs += d;
#pragma unroll
    for (int s = 0; s < 4; ++s) a.ka[s] += d;
    a.va[0] += d; a.va[1] += d;
    GAP();
  }
  if constexpr (N == 3) {
    if constexpr (NQ == 4) waitvm<MODE <= 12 ? 8 : MODE == 13 ? 4 : 6>();
    bar();
  }
  if constexpr (N >= 3 && N <= 6) { constexpr int f = N - 3; const unsigned ad = a.va[f & 1] + (f >> 1) * 8192; a.V[f >> 1][f & 1] = cat(ldstr(ad), ldstr(ad + 4096)); GAP(); }
  if constexpr (N >= 6) sm_op<NQ, (N - 6) / 7, (N - 6) % 7, NXT>(a);
#pragma unroll
  for (int t = 0; t < NQ; ++t)
#pragma unroll
    for (int ks = 0; ks < 2; ++ks)
      if (N == pv_start<NQ>(t) - (t == 0 ? 5 : 6) + ks) { a.P[t][ks] = lds128(a.pr + CUR + t * 2048 + ks * 1024); GAP(); }
  if constexpr (B == 2 * NQ - 4 && MODE != 14) { a.K[X] = lds128(a.ka[X]); GAP(); }
  if constexpr (NQ == 4) {
    if constexpr (MODE <= 13 && N >= 5 && N <= 14 && (N - 5) % 3 == 0) { constexpr int j = (N - 5) / 3; if (j == 0) set_m0(a.m0v); dma_o<j * 1024, true>(c.voff[j], c.srdV, (i + 2u) * SLOT); GAP(); }
    if constexpr (MODE == 0 && N >= 18 && N <= 27 && (N - 18) % 3 == 0) { constexpr int j = (N - 18) / 3; if (j == 0) set_m0(a.m0k); dma_o<j * 1024, true>(c.koff[j], c.srdK, (i + 4u) * SLOT); GAP(); }
    if constexpr ((MODE == 13 && (N == 18 || N == 26)) || (MODE == 14 && N == 5)) {
      constexpr int k = MODE == 14 ? 2 : (N - 18) / 8;
      const srd_t s = mksrd((const char*)c.wo + ((size_t)(c.h >> 3) * 1024u + c.wi * 32u) * (DM * 2u), 32 * DM * 2);
      asm volatile("buffer_load_dword %0, %1, %2, %3 offen" : "=&v"(a.warm[k]) : "v"(a.wv), "s"(s), "s"(k * 16384u) : "memory");
      GAP();
    }
    if constexpr (MODE == 14 && (N == 16 || N == 26)) { dma_b(c, 0, 2u * c.w4 + (N > 16)); GAP(); }
  }
}
template <int NQ, int PAR, int MODE, int... Ns> DI void step_(Ctx& c, Att<NQ>& a, unsigned i, std::integer_sequence<int, Ns...>) { (gap<NQ, PAR, MODE, Ns>(c, a, i), ...); }
template <int NQ, int PAR, int MODE> DI void step(Ctx& c, Att<NQ>& a, unsigned i) { step_<NQ, PAR, MODE>(c, a, i, std::make_integer_sequence<int, 8 * NQ>{}); }
template <int NQ, unsigned BUF, int LO, int... Ns> DI void sm_run(Att<NQ>& a, std::integer_sequence<int, Ns...>) { ((sm_op<NQ, (LO + Ns) / 7, (LO + Ns) % 7, BUF>(a)), ...); }

template <int NQ> DI void attend_project(Ctx& c, unsigned lane) {
  constexpr bool L = NQ == 4;
  const unsigned ldsb = c.ldsb, w = c.w, w4 = c.w4, h = c.h, b = c.b;
  constexpr unsigned T0 = L ? 5u : 0u;
  const srd_t srdO = mksrd(c.ws + (size_t)b * 9u * 131072u, 9 * 131072);
  {
    Att<NQ> a;
    {
      const unsigned r16 = lane & 15u, g4 = lane >> 4, q4 = r16 >> 2, p = lane & 3u;
#pragma unroll
      for (int s = 0; s < 4; ++s) a.ka[s] = ldsb + KR + (16u * w4 + r16) * 256u + (((4u * s + g4) ^ r16) << 4);
#pragma unroll
      for (int e = 0; e < 2; ++e) a.va[e] = ldsb + VR + SLOT + (4u * g4 + q4) * 256u + (((4u * w4 + 2u * e + (p >> 1)) ^ ((q4 << 2) | ((g4 & 1u) << 1))) << 4) + 8u * (p & 1u);
      a.pr = ldsb + PB + T0 * 2048u + lane * 16u;
      a.pw = a.pr + (w4 >> 1) * 1024u + (w4 & 1u) * 8u;
    }
#pragma unroll
    for (int s = 0; s < 4; ++s) a.K[s] = lds128(a.ka[s]);
#pragma unroll
    for (int t = 0; t < NQ; ++t)
#pragma unroll
      for (int s = 0; s < 4; ++s) a.Q[t][s] = lds128(ldsb + QI + (T0 + t) * 4096u + s * 1024u + lane * 16u);
    if constexpr (L) waitvm<4>();
    bar();
    if constexpr (L) { dma_k(c, 2); GAP(); }
#pragma unroll
    for (int t = 0; t < NQ; ++t) {
      mma0(a.S[t], a.K[0], a.Q[t][0]);
#pragma unroll
      for (int s = 1; s < 4; ++s) mma(a.S[t], a.K[s], a.Q[t][s]);
    }
#pragma unroll
    for (int s = 0; s < 4; ++s) { a.ka[s] += SLOT; a.K[s] = lds128(a.ka[s]); }
    a.rs = SLOT;
#pragma unroll
    for (int t = 0; t < NQ; ++t) { zero(a.O[t][0]); zero(a.O[t][1]); a.l[t] = 0.f; }
    GAP();
    sm_run<NQ, XSZ, 0>(a, std::make_integer_sequence<int, 8 * NQ - 6>{});
    if constexpr (L) { dma_v(c, 1); dma_k(c, 3); GAP(); }
    STAMP(3);
#pragma nounroll
    for (unsigned i = 0; i < (L ? 12u : 14u); i += 2u) {
      step<NQ, 0, 0>(c, a, i);
      step<NQ, 1, 0>(c, a, i + 1u);
      if (i == 2) STAMP(4); else if (i == 10) STAMP(5);
    }
    if constexpr (L) {
      { const unsigned l = fresh_lane(); set_boff(c, l); a.wv = (w4 * 64u + l) * 64u; GAP(); }
      step<NQ, 0, 12>(c, a, 12u);
      step<NQ, 1, 13>(c, a, 13u);
    }
    step<NQ, 0, 14>(c, a, 14u);
    asm volatile("s_nop 0");
    sm_run<NQ, 0u, 8 * NQ - 6>(a, std::make_integer_sequence<int, 6 - NQ>{});
    lane = fresh_lane();
    {
      const unsigned sa = ldsb + STATS + T0 * 256u + (lane & 15u) * 16u;
#pragma unroll
      for (int t = 0; t < NQ; ++t) {
        const float l = swap32_add(swap16_add(a.l[t]));
        if (lane < 16u) *(LAS float*)(sa + t * 256 + w4 * 4u) = l;
      }
      if constexpr (L) waitvm<9>(); else set_boff(c, lane);
      bar();
      dma_b(c, 1, w); GAP();
#pragma unroll
      for (int f = 0; f < 4; ++f) { const unsigned ad = a.va[f & 1] + SLOT + (f >> 1) * 8192; a.V[f >> 1][f & 1] = cat(ldstr(ad), ldstr(ad + 4096)); }
      f32x4 ls[NQ];
#pragma unroll
      for (int t = 0; t < NQ; ++t) { a.P[t][0] = lds128(a.pr + t * 2048); a.P[t][1] = lds128(a.pr + t * 2048 + 1024); }
#pragma unroll
      for (int t = 0; t < NQ; ++t) ls[t] = *(LAS f32x4*)(sa + t * 256);
      GAP();
#pragma unroll
      for (int t = 0; t < NQ; ++t)
#pragma unroll
        for (int x = 0; x < 4; ++x) mma(a.O[t][x & 1], a.V[x >> 1][x & 1], a.P[t][x >> 1]);
      mma_done();
      STAMP(6);
#pragma unroll
      for (int t = 0; t < NQ; ++t) {
        const float inv = __builtin_amdgcn_rcpf((ls[t][0] + ls[t][1]) + (ls[t][2] + ls[t][3]));
        const f32x4 o0 = a.O[t][0] * inv, o1 = a.O[t][1] * inv;
        const u32x4 f = {cvtpk(o0[0], o0[1]), cvtpk(o0[2], o0[3]), cvtpk(o1[0], o1[1]), cvtpk(o1[2], o1[3])};
        store_wt(f, srdO, lane * 16u, (T0 + t) * 131072u + (h * 4u + w4) * 1024u);
      }
      GAP();
      dma_b(c, 2, w); dma_b(c, 3, w);
      waitvm<6>();
      if constexpr (L) asm volatile("" ::"v"(a.warm[0]), "v"(a.warm[1]), "v"(a.warm[2]));
    }
  }
  TSTAMP(7);
  const unsigned kh = w >> 2, r16 = lane & 15u, g4 = lane >> 4, q4 = r16 >> 2, p = lane & 3u;
  const unsigned g = h >> 3, j = h & 7u;
  gu64* const flags = (gu64*)(c.ws + WS_FLAG);
  bar();
  if (!L && w == 0 && lane == 0) __hip_atomic_store(flags + (b * 32u + h) * 16u, c.tag, __ATOMIC_RELAXED, __HIP_MEMORY_SCOPE_AGENT);
  TSTAMP(8);
  unsigned ba[2][2], ao[2];
#pragma unroll
  for (int ks = 0; ks < 2; ++ks) {
    const unsigned ksa = ks ^ (w & 1u);
    ao[ks] = lane * 16u + (2u * kh + ksa) * 1024u;
#pragma unroll
    for (int e = 0; e < 2; ++e) ba[ks][e] = ldsb + (64u * kh + 32u * ksa + 4u * g4 + q4) * 192u + ((((j & 1u) + e) ^ (g4 & 1u)) - e) * 32u + 8u * p;
  }
  bf16x8 Bf[2][2][5], Af[2][2][2], Ar[2];
  auto read_b = [&](int par, int ks, int nt, unsigned slot) { const unsigned ad = ba[ks][nt & 1] + slot + nt * 32; Bf[par][ks][nt] = cat(ldstr(ad), ldstr(ad + 3072)); };
  auto a_off = [&](unsigned s) { return s < 8u ? (g * 8u + ((j + s) & 7u)) * 4096u : 0x40000000u; };
  auto load_a = [&](int par, int ks, unsigned so) {
#pragma unroll
    for (int qt = 0; qt < 2; ++qt) Af[par][ks][qt] = __builtin_bit_cast(bf16x8, __builtin_amdgcn_raw_buffer_load_b128(srdO, ao[ks], (2u * w4 + (qt ^ kh)) * 131072u + so, 16));
    if (!L && ks == 0) Ar[par] = __builtin_bit_cast(bf16x8, __builtin_amdgcn_raw_buffer_load_b128(srdO, ao[0], 8u * 131072u + so, 16));
  };
  load_a(0, 0, a_off(0)); load_a(0, 1, a_off(0));
  GAP();
#pragma unroll
  for (int par = 0; par < 2; ++par)
#pragma unroll
    for (int ks = 0; ks < 2; ++ks)
#pragma unroll
      for (int nt = 0; nt < 5; ++nt) read_b(par, ks, nt, bslot(par));
  GAP();
  dma_b(c, 4, w); dma_b(c, 5, w);
  f32x4 acc[2][5], accr[5];
#pragma unroll
  for (int nt = 0; nt < 5; ++nt) { zero(acc[0][nt]); zero(acc[1][nt]); if (!L) zero(accr[nt]); }
  bar();
  dma_b(c, 6, w); dma_b(c, 7, w);
  if (!L && w == 0) {
    gu64* f = flags + (b * 32u + g * 8u + (lane & 7u)) * 16u;
#pragma nounroll
    for (unsigned spin = 0; spin < (1u << 16); ++spin) {
      if (__all(__hip_atomic_load(f, __ATOMIC_RELAXED, __HIP_MEMORY_SCOPE_AGENT) == c.tag)) break;
      __builtin_amdgcn_s_sleep(1);
    }
  }
  TSTAMP(9);
  waitvm<0>();
  bar();
  TSTAMP(10);
  load_a(1, 0, a_off(1)); load_a(1, 1, a_off(1));
  GAP();
#pragma nounroll
  for (unsigned s = 0; s < 8u; s += 2u) {
#pragma unroll
    for (int par = 0; par < 2; ++par) {
      const unsigned sn = s + par + 2u, so = a_off(sn);
      const unsigned slot = sn == 2u ? 0u : sn == 6u ? bslot(6) : sn >= 7u ? BSZ : (sn - 1u) * BSZ;
#pragma unroll
      for (int ks = 0; ks < 2; ++ks) {
#pragma unroll
        for (int nt = 0; nt < 5; ++nt) {
          mma(acc[0][nt], Bf[par][ks][nt], Af[par][ks][0]);
          mma(acc[1][nt], Bf[par][ks][nt], Af[par][ks][1]);
          if (!L && ks == 0) mma(accr[nt], Bf[par][ks][nt], Ar[par]);
          read_b(par, ks, nt, slot); GAP();
        }
        load_a(par, ks, so); GAP();
      }
    }
  }
  mma_done();
  TSTAMP(11);
  bar();
#pragma unroll
  for (int nt = 0; nt < 5; ++nt) *(LAS f32x4*)(ldsb + w * 5120u + nt * 1024u + lane * 16u) = acc[1][nt];
  if constexpr (!L) {
#pragma unroll
    for (int nt = 0; nt < 5; ++nt) *(LAS f32x4*)(ldsb + 40960u + w4 * 5120u + nt * 1024u + lane * 16u) = accr[nt];
  }
  bar();
  const srd_t srdM = mksrd(c.ws + WS_MBOX + (size_t)b * 32u * MBOX, 32 * MBOX);
  {
    const unsigned vo = q4 * (8u * (unsigned)MBOX) + (g4 * 4u + p) * 16u, so = j * (unsigned)MBOX + g * 11520u;
#pragma unroll
    for (int nt = 0; nt < 5; ++nt) {
      const f32x4 v = acc[0][nt] + *(LAS f32x4*)(ldsb + (w ^ 4u) * 5120u + nt * 1024u + lane * 16u);
      store_wt(__builtin_bit_cast(u32x4, v), srdM, vo, so + ((2u * w4 + kh) * 5u + nt) * 256u);
    }
    if constexpr (L) {
#pragma nounroll
      for (unsigned nt = w4; nt < 5u; nt += 4u) {
        f32x4 v = kZero4;
#pragma unroll
        for (int i = 0; i < 4; ++i) v += *(LAS f32x4*)(ldsb + 40960u + i * 5120u + nt * 1024u + lane * 16u);
        store_wt(__builtin_bit_cast(u32x4, v), srdM, vo, so + (40u + nt) * 256u);
      }
    }
  }
  TSTAMP(12);
  waitvm<0>();
  bar();
  if (!L && w == 0) {
    if (lane == 0) __hip_atomic_store(flags + (256u + b * 32u + h) * 16u, c.tag, __ATOMIC_RELAXED, __HIP_MEMORY_SCOPE_AGENT);
    TSTAMP(13);
    gu64* f = flags + (256u + b * 32u + (lane & 3u) * 8u + j) * 16u;
#pragma nounroll
    for (unsigned spin = 0; spin < (1u << 16); ++spin) {
      if (__all(__hip_atomic_load(f, __ATOMIC_RELAXED, __HIP_MEMORY_SCOPE_AGENT) == c.tag)) break;
      __builtin_amdgcn_s_sleep(1);
    }
    if (h == 0 && lane == 0) __hip_atomic_store(flags + (512u + b) * 16u, c.tag, __ATOMIC_RELAXED, __HIP_MEMORY_SCOPE_AGENT);
  }
  bar();
  TSTAMP(14);
  {
    f32x4 v[2][4];
#pragma unroll
    for (int k = 0; k < 2; ++k)
#pragma unroll
      for (int i = 0; i < 4; ++i) {
        const unsigned u = w * 64u + lane + 512u * k;
        v[k][i] = __builtin_bit_cast(f32x4, __builtin_amdgcn_raw_buffer_load_b128(srdM, u < 720u ? u * 16u : 0x40000000u, h * (unsigned)MBOX + i * 11520u, 16));
      }
#pragma unroll
    for (int k = 0; k < 2; ++k) {
      const unsigned u = w * 64u + lane + 512u * k;
      const f32x4 r = (v[k][0] + v[k][1]) + (v[k][2] + v[k][3]);
      const unsigned t = u >> 4, mt = t / 5u, nt = t - 5u * mt;
      if (u < 720u) *(u32x2*)(c.out + (size_t)(b * SQ + 16u * mt + 4u * g + (u & 3u)) * DM + 80u * j + 16u * nt + ((u >> 2) & 3u) * 4u) = u32x2{cvtpk(r[0], r[1]), cvtpk(r[2], r[3])};
    }
  }
  STAMP_END;
}

__global__ __launch_bounds__(512, 2) void fused_kernel(const u16* __restrict__ x, const u16* __restrict__ kc, const u16* __restrict__ vc,
                                                       const u16* __restrict__ wq, const u16* __restrict__ wo, char* ws, u16* __restrict__ out) {
  extern __shared__ __attribute__((aligned(16))) char lds[];
  unsigned long long pc; asm volatile("s_getpc_b64 %0" : "=s"(pc));
  Ctx c;
  _Pragma("unroll") for (int i = 0; i < 16; ++i) { c.st.t[i] = 0; c.st.c[i] = 0; }
  STAMP(0);
  const unsigned ldsb = (unsigned)(size_t)(LAS char*)lds;
  const unsigned lane = threadIdx.x & 63u;
  const unsigned w = rfl(threadIdx.x >> 6);
  const unsigned bid = blockIdx.x, xg = bid & 7u, wi = bid >> 3;
  const unsigned h = (xg & 3u) * 8u + (wi & 7u), b = (xg >> 2) * 4u + (wi >> 3);
  const unsigned w4 = w & 3u;
  const bool loader = ((w + 2u) & 4u) != 0u;
  const unsigned r16 = lane & 15u, g4 = lane >> 4, q4 = r16 >> 2, p = lane & 3u;
  const unsigned wsw = (q4 << 2) | ((g4 & 1u) << 1);
  c.ldsb = ldsb; c.w = w; c.w4 = w4; c.b = b; c.h = h; c.wi = wi; c.ws = ws; c.out = out; c.wo = wo; c.tag = 0;
  c.srdB = mksrd((const char*)wo + (size_t)(h >> 3) * 1024u * DM * 2u + (h & 7u) * 160u - (h & 1u) * 32u, 1023 * DM * 2 + 192);

  if (loader) {
    const srd_t srdX = mksrd(x + (size_t)b * SQ * DM, SQ * DM * 2);
    const srd_t srdW = mksrd(wq + h * DH, (DM * HD - h * DH) * 2);
    unsigned xoff[4], woff[4];
#pragma unroll
    for (int i = 0; i < 4; ++i) {
      xoff[i] = (32u * w4 + 8u * i + (lane >> 3)) * (DM * 2u) + (((lane & 7u) ^ (4u * (i & 1) + g4)) << 4) - i * 1024u;
      woff[i] = (16u * w4 + 4u * i + g4) * (HD * 2u) + ((r16 ^ ((g4 << 2) | (i & 2))) << 4) - i * 1024u;
    }
    const unsigned xoff4 = (128u + 4u * w4 + (lane >> 3)) * (DM * 2u) + (((lane & 7u) ^ ((2u * w4 + g4) & 7u)) << 4);
    unsigned kk = ROT;
    auto dma_stage = [&](unsigned wsl, unsigned xsl) {
      dma4<false>(ldsb + WS0 + wsl + w4 * 4096u, woff, srdW, kk * (64u * HD * 2u));
      dma4<false>(ldsb + XS0 + xsl + w4 * 4096u, xoff, srdX, kk * 128u);
      dma_half(ldsb + XS0 + xsl + 16384u + w4 * 512u, xoff4, srdX, kk * 128u);
      kk = kk == 9u ? 0u : kk + 1u;
    };
    QSTAMP(7);
    dma_stage(2u * SLOT, 0u);
    QSTAMP(8);
    dma_stage(0u, XSZ);
    QSTAMP(9);
    c.srdK = mksrd(kc + (size_t)(b * NH + h) * SK * DH, SK * DH * 2);
    c.srdV = mksrd(vc + (size_t)(b * NH + h) * SK * DH, SK * DH * 2);
#pragma unroll
    for (int i = 0; i < 4; ++i) {
      c.koff[i] = (16u * w4 + g4) * 256u + ((r16 ^ (4u * i + g4)) << 4);
      c.voff[i] = (16u * w4 + g4) * 256u + ((r16 ^ ((g4 << 2) | ((i & 1) << 1))) << 4);
    }
    f32x4 acc[2] = {kZero4, kZero4};
    unsigned xa[2], wa[2];
#pragma unroll
    for (int s = 0; s < 2; ++s) xa[s] = ldsb + XS0 + (128u + r16) * 128u + (((4u * s + g4) ^ (r16 >> 1)) << 4);
#pragma unroll
    for (int e = 0; e < 2; ++e) wa[e] = ldsb + WS0 + (8u * g4 + q4) * 256u + (((4u * w4 + 2u * e + (p >> 1)) ^ wsw) << 4) + 8u * (p & 1u);
    bf16x8 xf[2], wf[2][2];
    xf[0] = xf[1] = wf[0][0] = wf[0][1] = wf[1][0] = wf[1][1] = bf16x8{};
    auto mm = [&]() {
#pragma unroll
      for (int s = 0; s < 2; ++s)
#pragma unroll
        for (int e = 0; e < 2; ++e) acc[e] = __builtin_amdgcn_mfma_f32_16x16x32_bf16(wf[s][e], xf[s], acc[e], 0, 0, 0);
      GAP();
    };
    auto stage = [&](unsigned wsl, unsigned xsl, unsigned wprev, unsigned xprev, int kind) {
      if (kind == 2) waitvm<4>(); else waitvm<9>();
      bar();
      mm();
#pragma unroll
      for (int s = 0; s < 2; ++s) {
        xf[s] = lds128(xa[s] + xsl);
#pragma unroll
        for (int e = 0; e < 2; ++e) { const unsigned ad = wa[e] + wsl + s * 8192; wf[s][e] = cat(ldstr(ad), ldstr(ad + 1024)); }
      }
      GAP();
      if (kind == 0) dma_stage(wprev, xprev); else dma_k(c, kind - 1);
      GAP();
    };
    __builtin_amdgcn_s_setprio(1);
    stage(2u * SLOT, 0u, SLOT, 2u * XSZ, 0);
    QSTAMP(12);
    stage(0u, XSZ, 2u * SLOT, 0u, 0);
#pragma nounroll
    for (unsigned it = 0; it < 2u; ++it) {
      stage(SLOT, 2u * XSZ, 0u, XSZ, 0);
      stage(2u * SLOT, 0u, SLOT, 2u * XSZ, 0);
      if (it == 0) QSTAMP(13); else QSTAMP(14);
      stage(0u, XSZ, 2u * SLOT, 0u, 0);
    }
    stage(SLOT, 2u * XSZ, 0u, 0u, 1);
    stage(2u * SLOT, 0u, 0u, 0u, 2);
    bar();
    mm();
    __builtin_amdgcn_s_setprio(0);
#pragma unroll
    for (int e = 0; e < 2; ++e) {
      u32x2 q = {cvtpk(acc[e][0] * QSCALE, acc[e][1] * QSCALE), cvtpk(acc[e][2] * QSCALE, acc[e][3] * QSCALE)};
      *(LAS u32x2*)(ldsb + QI + 8u * 4096u + w4 * 1024u + ((2u * e + (g4 >> 1)) * 16u + r16) * 16u + (g4 & 1u) * 8u) = q;
    }
    STAMP(1);
    waitvm<4>();
    bar();
    dma_v(c, 0);
    STAMP(2);
    attend_project<4>(c, lane);
  } else {
    if (w == 0) {
      const unsigned long long e = __hip_atomic_load((gu64*)(ws + WS_FLAG) + (512u + b) * 16u, __ATOMIC_RELAXED, __HIP_MEMORY_SCOPE_AGENT) + 1ull;
      c.tag = ((unsigned long long)rfl((unsigned)(e >> 32)) << 32) | rfl((unsigned)e);
    }
    if (w == 1) {
      const srd_t srdC = mksrd((const void*)(pc & ~255ull), CODE_LINES * 128);
      unsigned ln = wi * (CODE_LINES / 32) + (lane >> 1); ln = ln >= CODE_LINES ? ln - CODE_LINES : ln;
      unsigned dummy;
      asm volatile("s_nop 4\n\tbuffer_load_dword %0, %1, %2, 0 offen\n\ts_waitcnt vmcnt(0)" : "=&v"(dummy) : "v"(ln * 128u + (lane & 1u) * 64u), "s"(srdC) : "memory");
    }
    const unsigned qh = w4 & 1u, dh = w4 >> 1;
    f32x4 acc[4][4];
#pragma unroll
    for (int dt = 0; dt < 4; ++dt)
#pragma unroll
      for (int qt = 0; qt < 4; ++qt) zero(acc[dt][qt]);
    unsigned xa[2], wa[4];
#pragma unroll
    for (int s = 0; s < 2; ++s) xa[s] = ldsb + XS0 + (64u * qh + r16) * 128u + (((4u * s + g4) ^ (r16 >> 1)) << 4);
#pragma unroll
    for (int dt = 0; dt < 4; ++dt) wa[dt] = ldsb + WS0 + 2u * SLOT + (8u * g4 + q4) * 256u + (((8u * dh + 2u * dt + (p >> 1)) ^ wsw) << 4) + 8u * (p & 1u);
    bf16x8 xf[2][4];
    bf16x4 wl[2][4], wu[2][4];
    auto rd = [&](int s, int r) {
      if (r == 0) wl[s][0] = ldstr(wa[0] + s * 8192);
      else if (r == 1) wu[s][0] = ldstr(wa[0] + s * 8192 + 1024);
      else if (r < 6) xf[s][r - 2] = lds128(xa[s] + (r - 2) * 2048);
      else if (r & 1) wu[s][(r - 4) >> 1] = ldstr(wa[(r - 4) >> 1] + s * 8192 + 1024);
      else wl[s][(r - 4) >> 1] = ldstr(wa[(r - 4) >> 1] + s * 8192);
      GAP();
    };
    bar();
#pragma unroll
    for (int r = 0; r < 12; ++r) rd(0, r);
    unsigned sl = 0, dx = XSZ, dw = -2 * (int)SLOT;
#pragma nounroll
    for (unsigned sg = 0; sg < 10u; ++sg) {
#pragma unroll
      for (int m = 0; m < 16; ++m) {
        mma(acc[m >> 2][m & 3], cat(wl[0][m >> 2], wu[0][m >> 2]), xf[0][m & 3]);
        if (m < 12) rd(1, m);
        if (m == 12) { xa[0] += dx; xa[1] += dx; GAP(); }
        if (m == 13) { wa[0] += dw; wa[1] += dw; GAP(); }
        if (m == 14) { wa[2] += dw; wa[3] += dw; GAP(); }
      }
#pragma unroll
      for (int m = 0; m < 16; ++m) {
        mma(acc[m >> 2][m & 3], cat(wl[1][m >> 2], wu[1][m >> 2]), xf[1][m & 3]);
        if (m == 0) bar();
        if (m < 12) rd(0, m);
        if (m == 13) { sl = sl == 2u ? 0u : sl + 1u; asm volatile("" : "+s"(sl)); GAP(); }
        if (m == 14) { dx = sl == 2u ? -2 * (int)XSZ : XSZ; asm volatile("" : "+s"(dx)); GAP(); }
        if (m == 15) { dw = sl == 0u ? -2 * (int)SLOT : SLOT; asm volatile("" : "+s"(dw)); GAP(); }
      }
    }
    mma_done();
#pragma unroll
    for (int dt = 0; dt < 4; ++dt)
#pragma unroll
      for (int qt = 0; qt < 4; ++qt) {
        u32x2 q = {cvtpk(acc[dt][qt][0] * QSCALE, acc[dt][qt][1] * QSCALE), cvtpk(acc[dt][qt][2] * QSCALE, acc[dt][qt][3] * QSCALE)};
        *(LAS u32x2*)(ldsb + QI + (4u * qh + qt) * 4096u + (2u * dh + (dt >> 1)) * 1024u + ((2u * (dt & 1) + (g4 >> 1)) * 16u + r16) * 16u + (g4 & 1u) * 8u) = q;
      }
    STAMP(1);
    bar();
    STAMP(2);
    attend_project<5>(c, lane);
  }
}

extern "C" void kernel_launch(void* const* d_in, const int* in_sizes, int n_in, void* d_out, int out_size, void* d_ws, size_t ws_size, hipStream_t stream) {
  (void)hipFuncSetAttribute((const void*)fused_kernel, hipFuncAttributeMaxDynamicSharedMemorySize, LDS_A);
  fused_kernel<<<256, 512, LDS_A, stream>>>((const u16*)d_in[0], (const u16*)d_in[1], (const u16*)d_in[2], (const u16*)d_in[3], (const u16*)d_in[4], (char*)d_ws, (u16*)d_out);
}
```

```cpp
#include <hip/hip_runtime.h>
#include <hip/hip_bf16.h>
#include <utility>

typedef unsigned short u16;
typedef float f32x4 __attribute__((ext_vector_type(4)));
typedef float f32x2 __attribute__((ext_vector_type(2)));
typedef __bf16 bf16x8 __attribute__((ext_vector_type(8)));
typedef __bf16 bf16x4 __attribute__((ext_vector_type(4)));
typedef __bf16 bf16x2 __attribute__((ext_vector_type(2)));
typedef unsigned u32x4 __attribute__((ext_vector_type(4)));
typedef unsigned u32x2 __attribute__((ext_vector_type(2)));
typedef __attribute__((address_space(1))) unsigned long long gu64;
typedef __amdgpu_buffer_rsrc_t srd_t;
#define LAS __attribute__((address_space(3)))
#define DI __device__ __forceinline__
#define GAP() __builtin_amdgcn_sched_barrier(0)

constexpr int SQ = 144, DM = 640, NH = 32, SK = 1024, DH = 128;
constexpr int HD = NH * DH;
constexpr float QSCALE = 0.12751743082f;
constexpr unsigned CODE_LINES = 192;

#define STAMPS 0

constexpr unsigned SLOT = 16384;
constexpr unsigned KR = 0, VR = 49152;
constexpr unsigned WS0 = 32768;
constexpr unsigned XS0 = 98304, XSZ = 18432;
constexpr unsigned QI = XS0 + XSZ;
constexpr unsigned PB = 98304;
constexpr unsigned STATS = 159744;
constexpr unsigned BSZ = 24576;
constexpr unsigned LDS_A = 163840;
constexpr unsigned bslot(int s) { return (s == 0 || s == 6) ? 135168u : (s == 1 || s == 7) ? BSZ : s == 2 ? 0u : (s - 1) * BSZ; }
constexpr size_t WS_MBOX = 9437184, MBOX = 46080;
constexpr size_t WS_FLAG = WS_MBOX + 256 * MBOX;

DI unsigned rfl(unsigned x) { return __builtin_amdgcn_readfirstlane(x); }
DI unsigned cvtpk(float lo, float hi) { f32x2 v = {lo, hi}; bf16x2 b = __builtin_convertvector(v, bf16x2); return __builtin_bit_cast(unsigned, b); }
DI bf16x8 lds128(unsigned a) { return *(LAS bf16x8*)a; }
DI bf16x4 ldstr(unsigned a) { return __builtin_amdgcn_ds_read_tr16_b64_v4bf16((LAS bf16x4*)a); }
DI bf16x8 cat(bf16x4 a, bf16x4 b) { return __builtin_shufflevector(a, b, 0, 1, 2, 3, 4, 5, 6, 7); }
DI srd_t mksrd(const void* p, unsigned bytes) { return __builtin_amdgcn_make_buffer_rsrc((void*)p, (short)0, bytes, 0x00020000); }
template <int N> DI void waitvm() { asm volatile("s_waitcnt vmcnt(%0)" ::"i"(N) : "memory"); }
DI void bar() { asm volatile("s_waitcnt lgkmcnt(0)\n\ts_barrier" ::: "memory"); GAP(); }
DI void dma(unsigned m0, unsigned voff, srd_t srd, unsigned soff) {
  asm volatile("s_mov_b32 m0, %0\n\ts_nop 0\n\tbuffer_load_dwordx4 %1, %2, %3 offen lds" ::"s"(m0), "v"(voff), "s"(srd), "s"(soff) : "memory");
}
DI void dma_nt(unsigned m0, unsigned voff, srd_t srd, unsigned soff) {
  asm volatile("s_mov_b32 m0, %0\n\ts_nop 0\n\tbuffer_load_dwordx4 %1, %2, %3 offen nt lds" ::"s"(m0), "v"(voff), "s"(srd), "s"(soff) : "memory");
}
DI void set_m0(unsigned m0) { asm volatile("s_mov_b32 m0, %0\n\ts_nop 0" ::"s"(m0) : "memory"); }
template <int OFF, bool NT> DI void dma_o(unsigned voff, srd_t srd, unsigned soff) {
  if constexpr (NT) asm volatile("buffer_load_dwordx4 %0, %1, %2 offen offset:%3 nt lds" ::"v"(voff), "s"(srd), "s"(soff), "i"(OFF) : "memory");
  else asm volatile("buffer_load_dwordx4 %0, %1, %2 offen offset:%3 lds" ::"v"(voff), "s"(srd), "s"(soff), "i"(OFF) : "memory");
}
template <bool NT> DI void dma4(unsigned m0, const unsigned (&voff)[4], srd_t srd, unsigned soff) {
  set_m0(m0); dma_o<0, NT>(voff[0], srd, soff); dma_o<1024, NT>(voff[1], srd, soff); dma_o<2048, NT>(voff[2], srd, soff); dma_o<3072, NT>(voff[3], srd, soff);
}
DI void dma_half(unsigned m0, unsigned voff, srd_t srd, unsigned soff) {
  asm volatile("s_mov_b32 m0, %0\n\ts_mov_b32 exec_hi, 0\n\tbuffer_load_dwordx4 %1, %2, %3 offen lds\n\ts_mov_b32 exec_hi, -1" ::"s"(m0), "v"(voff), "s"(srd), "s"(soff) : "memory");
}
DI void store_wt(u32x4 v, srd_t srd, unsigned voff, unsigned soff) { __builtin_amdgcn_raw_buffer_store_b128(v, srd, voff, soff, 16); GAP(); asm volatile("s_nop 1"); GAP(); }
DI float swap32_add(float x) { auto r = __builtin_amdgcn_permlane32_swap(__float_as_uint(x), __float_as_uint(x), false, false); return __uint_as_float(r[0]) + __uint_as_float(r[1]); }
DI float swap16_add(float x) { auto r = __builtin_amdgcn_permlane16_swap(__float_as_uint(x), __float_as_uint(x), false, false); return __uint_as_float(r[0]) + __uint_as_float(r[1]); }
DI float fadd(float a, float b) { float r; asm("v_add_f32_e32 %0, %1, %2" : "=v"(r) : "v"(a), "v"(b)); return r; }
DI unsigned fresh_lane() { unsigned l; asm volatile("v_mbcnt_lo_u32_b32 %0, -1, 0\n\tv_mbcnt_hi_u32_b32 %0, -1, %0" : "=v"(l)); return l; }
DI void mma(f32x4& d, bf16x8 a, bf16x8 b) { asm volatile("v_mfma_f32_16x16x32_bf16 %0, %1, %2, %0" : "+v"(d) : "v"(a), "v"(b)); GAP(); }
DI void mma0(f32x4& d, bf16x8 a, bf16x8 b) { asm volatile("v_mfma_f32_16x16x32_bf16 %0, %1, %2, 0" : "=&v"(d) : "v"(a), "v"(b)); GAP(); }
DI void mma_done() { GAP(); asm volatile("s_nop 7"); GAP(); }
const f32x4 kZero4 = {0.f, 0.f, 0.f, 0.f};
DI void zero(f32x4& d) { d = kZero4; asm volatile("" : "+v"(d)); }

#define STAMP(i) do {} while (0)
#define STAMP_END do {} while (0)

#define QSTAMP(i) do {} while (0)
#define TSTAMP(i) STAMP(i)
#define ROT 0u
struct Ctx {
  unsigned ldsb, w, w4, b, h, wi;
  unsigned long long tag;
  char* ws; u16* out; const u16* wo;
  srd_t srdK, srdV, srdB;
  unsigned koff[4], voff[4], boff[3];
};
DI void dma_k(const Ctx& c, unsigned t) { dma4<true>(c.ldsb + KR + (t % 3u) * SLOT + c.w4 * 4096u, c.koff, c.srdK, t * SLOT); }
DI void dma_v(const Ctx& c, unsigned t) { dma4<true>(c.ldsb + VR + ((t + 2u) % 3u) * SLOT + c.w4 * 4096u, c.voff, c.srdV, t * SLOT); }
DI void set_boff(Ctx& c, unsigned lane) {
#pragma unroll
  for (int i = 0; i < 3; ++i) { const unsigned qd = 16u * i + (lane >> 2), row = qd / 3u; c.boff[i] = row * (DM * 2u) + (qd - 3u * row) * 64u + (((lane & 3u) ^ (((row >> 2) & 1u) << 1)) << 4) - i * 1024u; }
}
DI void dma_b(const Ctx& c, int s, unsigned rb) {
  const unsigned m = c.ldsb + bslot(s) + rb * 3072u, so = ((((c.h + s) & 7u) << 7) + rb * 16u) * (DM * 2u);
  set_m0(m); dma_o<0, false>(c.boff[0], c.srdB, so); dma_o<1024, false>(c.boff[1], c.srdB, so); dma_o<2048, false>(c.boff[2], c.srdB, so);
}

template <int NQ> struct Att {
  f32x4 O[NQ][2], S[NQ];
  bf16x8 Q[NQ][4], K[4], V[2][2], P[NQ][2];
  u32x2 pk;
  float l[NQ];
  unsigned ka[4], va[2], pw, pr;
  unsigned rs, m0k, m0v;
  unsigned warm[3], wv;
};
template <int NQ> constexpr bool blk_pv(int b) { return b >= 2 * NQ - 3 ? true : (b >= 3 && (b & 1)); }
template <int NQ> constexpr int blk_tile(int b) { return b >= 2 * NQ - 3 ? b - NQ : b < 3 ? b : (b & 1) ? (b - 3) / 2 : (b + 2) / 2; }
template <int NQ> constexpr int pv_start(int t) { for (int b = 0; b < 2 * NQ; ++b) if (blk_pv<NQ>(b) && blk_tile<NQ>(b) == t) return 4 * b; return -1; }
template <int NQ, int T, int F, unsigned BUF> DI void sm_op(Att<NQ>& a) {
  if constexpr (F == 0 || F == 1) a.S[T][F] = __builtin_amdgcn_exp2f(a.S[T][F]);
  if constexpr (F == 3 || F == 4) a.S[T][F - 1] = __builtin_amdgcn_exp2f(a.S[T][F - 1]);
  if constexpr (F == 2) { a.l[T] = fadd(a.l[T], a.S[T][0]); GAP(); a.pk[0] = cvtpk(a.S[T][0], a.S[T][1]); }
  if constexpr (F == 5) { a.l[T] = fadd(a.l[T], a.S[T][1]); GAP(); a.pk[1] = cvtpk(a.S[T][2], a.S[T][3]); }
  if constexpr (F == 6) { *(LAS u32x2*)(a.pw + BUF + T * 2048) = a.pk; GAP(); a.l[T] = fadd(a.l[T], a.S[T][2]); GAP(); a.l[T] = fadd(a.l[T], a.S[T][3]); }
  GAP();
}
template <int NQ, int PAR, int MODE, int N> DI void gap(Ctx& c, Att<NQ>& a, unsigned i) {
  constexpr int G = 8 * NQ, B = N >> 2, X = N & 3, T = blk_tile<NQ>(B);
  constexpr unsigned CUR = PAR ? 0u : XSZ, NXT = PAR ? XSZ : 0u;
  if constexpr (blk_pv<NQ>(B)) mma(a.O[T][X & 1], a.V[X >> 1][X & 1], a.P[T][X >> 1]);
  else if constexpr (X == 0) mma0(a.S[T], a.K[0], a.Q[T][0]);
  else mma(a.S[T], a.K[X], a.Q[T][X]);
  if constexpr (N + G - 6 < 7 * NQ) sm_op<NQ, NQ - 1, N + G - 6 - 7 * (NQ - 1), CUR>(a);
  if constexpr (N == 2) {
    asm volatile("" : "+s"(a.rs));
    if constexpr (NQ == 4) { a.m0k = c.ldsb + KR + c.w4 * 4096u + a.rs; a.m0v = a.m0k + (VR - KR); }
    const unsigned d = a.rs == 2u * SLOT ? -2 * (int)SLOT : SLOT;
    a.rs += d;
#pragma unroll
    for (int s = 0; s < 4; ++s) a.ka[s] += d;
    a.va[0] += d; a.va[1] += d;
    GAP();
  }
  if constexpr (N == 3) {
    if constexpr (NQ == 4) waitvm<MODE <= 12 ? 8 : MODE == 13 ? 4 : 6>();
    bar();
  }
  if constexpr (N >= 3 && N <= 6) { constexpr int f = N - 3; const unsigned ad = a.va[f & 1] + (f >> 1) * 8192; a.V[f >> 1][f & 1] = cat(ldstr(ad), ldstr(ad + 4096)); GAP(); }
  if constexpr (N >= 6) sm_op<NQ, (N - 6) / 7, (N - 6) % 7, NXT>(a);
#pragma unroll
  for (int t = 0; t < NQ; ++t)
#pragma unroll
    for (int ks = 0; ks < 2; ++ks)
      if (N == pv_start<NQ>(t) - (t == 0 ? 5 : 6) + ks) { a.P[t][ks] = lds128(a.pr + CUR + t * 2048 + ks * 1024); GAP(); }
  if constexpr (B == 2 * NQ - 4 && MODE != 14) { a.K[X] = lds128(a.ka[X]); GAP(); }
  if constexpr (NQ == 4) {
    if constexpr (MODE <= 13 && N >= 5 && N <= 14 && (N - 5) % 3 == 0) { constexpr int j = (N - 5) / 3; if (j == 0) set_m0(a.m0v); dma_o<j * 1024, true>(c.voff[j], c.srdV, (i + 2u) * SLOT); GAP(); }
    if constexpr (MODE == 0 && N >= 18 && N <= 27 && (N - 18) % 3 == 0) { constexpr int j = (N - 18) / 3; if (j == 0) set_m0(a.m0k); dma_o<j * 1024, true>(c.koff[j], c.srdK, (i + 4u) * SLOT); GAP(); }
    if constexpr ((MODE == 13 && (N == 18 || N == 26)) || (MODE == 14 && N == 5)) {
      constexpr int k = MODE == 14 ? 2 : (N - 18) / 8;
      const srd_t s = mksrd((const char*)c.wo + ((size_t)(c.h >> 3) * 1024u + c.wi * 32u) * (DM * 2u), 32 * DM * 2);
      asm volatile("buffer_load_dword %0, %1, %2, %3 offen" : "=&v"(a.warm[k]) : "v"(a.wv), "s"(s), "s"(k * 16384u) : "memory");
      GAP();
    }
    if constexpr (MODE == 14 && (N == 16 || N == 26)) { dma_b(c, 0, 2u * c.w4 + (N > 16)); GAP(); }
  }
}
template <int NQ, int PAR, int MODE, int... Ns> DI void step_(Ctx& c, Att<NQ>& a, unsigned i, std::integer_sequence<int, Ns...>) { (gap<NQ, PAR, MODE, Ns>(c, a, i), ...); }
template <int NQ, int PAR, int MODE> DI void step(Ctx& c, Att<NQ>& a, unsigned i) { step_<NQ, PAR, MODE>(c, a, i, std::make_integer_sequence<int, 8 * NQ>{}); }
template <int NQ, unsigned BUF, int LO, int... Ns> DI void sm_run(Att<NQ>& a, std::integer_sequence<int, Ns...>) { ((sm_op<NQ, (LO + Ns) / 7, (LO + Ns) % 7, BUF>(a)), ...); }

template <int NQ> DI void attend_project(Ctx& c, unsigned lane) {
  constexpr bool L = NQ == 4;
  const unsigned ldsb = c.ldsb, w = c.w, w4 = c.w4, h = c.h, b = c.b;
  constexpr unsigned T0 = L ? 5u : 0u;
  const srd_t srdO = mksrd(c.ws + (size_t)b * 9u * 131072u, 9 * 131072);
  {
    Att<NQ> a;
    {
      const unsigned r16 = lane & 15u, g4 = lane >> 4, q4 = r16 >> 2, p = lane & 3u;
#pragma unroll
      for (int s = 0; s < 4; ++s) a.ka[s] = ldsb + KR + (16u * w4 + r16) * 256u + (((4u * s + g4) ^ r16) << 4);
#pragma unroll
      for (int e = 0; e < 2; ++e) a.va[e] = ldsb + VR + SLOT + (4u * g4 + q4) * 256u + (((4u * w4 + 2u * e + (p >> 1)) ^ ((q4 << 2) | ((g4 & 1u) << 1))) << 4) + 8u * (p & 1u);
      a.pr = ldsb + PB + T0 * 2048u + lane * 16u;
      a.pw = a.pr + (w4 >> 1) * 1024u + (w4 & 1u) * 8u;
    }
#pragma unroll
    for (int s = 0; s < 4; ++s) a.K[s] = lds128(a.ka[s]);
#pragma unroll
    for (int t = 0; t < NQ; ++t)
#pragma unroll
      for (int s = 0; s < 4; ++s) a.Q[t][s] = lds128(ldsb + QI + (T0 + t) * 4096u + s * 1024u + lane * 16u);
    if constexpr (L) waitvm<4>();
    bar();
    if constexpr (L) { dma_k(c, 2); GAP(); }
#pragma unroll
    for (int t = 0; t < NQ; ++t) {
      mma0(a.S[t], a.K[0], a.Q[t][0]);
#pragma unroll
      for (int s = 1; s < 4; ++s) mma(a.S[t], a.K[s], a.Q[t][s]);
    }
#pragma unroll
    for (int s = 0; s < 4; ++s) { a.ka[s] += SLOT; a.K[s] = lds128(a.ka[s]); }
    a.rs = SLOT;
#pragma unroll
    for (int t = 0; t < NQ; ++t) { zero(a.O[t][0]); zero(a.O[t][1]); a.l[t] = 0.f; }
    GAP();
    sm_run<NQ, XSZ, 0>(a, std::make_integer_sequence<int, 8 * NQ - 6>{});
    if constexpr (L) { dma_v(c, 1); dma_k(c, 3); GAP(); }
    STAMP(3);
#pragma nounroll
    for (unsigned i = 0; i < (L ? 12u : 14u); i += 2u) {
      step<NQ, 0, 0>(c, a, i);
      step<NQ, 1, 0>(c, a, i + 1u);
    }
    if constexpr (L) {
      { const unsigned l = fresh_lane(); set_boff(c, l); a.wv = (w4 * 64u + l) * 64u; GAP(); }
      step<NQ, 0, 12>(c, a, 12u);
      step<NQ, 1, 13>(c, a, 13u);
    }
    step<NQ, 0, 14>(c, a, 14u);
    asm volatile("s_nop 0");
    sm_run<NQ, 0u, 8 * NQ - 6>(a, std::make_integer_sequence<int, 6 - NQ>{});
    lane = fresh_lane();
    {
      const unsigned sa = ldsb + STATS + T0 * 256u + (lane & 15u) * 16u;
#pragma unroll
      for (int t = 0; t < NQ; ++t) {
        const float l = swap32_add(swap16_add(a.l[t]));
        if (lane < 16u) *(LAS float*)(sa + t * 256 + w4 * 4u) = l;
      }
      if constexpr (L) waitvm<9>(); else set_boff(c, lane);
      bar();
      dma_b(c, 1, w); GAP();
#pragma unroll
      for (int f = 0; f < 4; ++f) { const unsigned ad = a.va[f & 1] + SLOT + (f >> 1) * 8192; a.V[f >> 1][f & 1] = cat(ldstr(ad), ldstr(ad + 4096)); }
      f32x4 ls[NQ];
#pragma unroll
      for (int t = 0; t < NQ; ++t) { a.P[t][0] = lds128(a.pr + t * 2048); a.P[t][1] = lds128(a.pr + t * 2048 + 1024); }
#pragma unroll
      for (int t = 0; t < NQ; ++t) ls[t] = *(LAS f32x4*)(sa + t * 256);
      GAP();
#pragma unroll
      for (int t = 0; t < NQ; ++t)
#pragma unroll
        for (int x = 0; x < 4; ++x) mma(a.O[t][x & 1], a.V[x >> 1][x & 1], a.P[t][x >> 1]);
      mma_done();
      STAMP(6);
#pragma unroll
      for (int t = 0; t < NQ; ++t) {
        const float inv = __builtin_amdgcn_rcpf((ls[t][0] + ls[t][1]) + (ls[t][2] + ls[t][3]));
        const f32x4 o0 = a.O[t][0] * inv, o1 = a.O[t][1] * inv;
        const u32x4 f = {cvtpk(o0[0], o0[1]), cvtpk(o0[2], o0[3]), cvtpk(o1[0], o1[1]), cvtpk(o1[2], o1[3])};
        store_wt(f, srdO, lane * 16u, (T0 + t) * 131072u + (h * 4u + w4) * 1024u);
      }
      GAP();
      dma_b(c, 2, w); dma_b(c, 3, w);
      waitvm<6>();
      if constexpr (L) asm volatile("" ::"v"(a.warm[0]), "v"(a.warm[1]), "v"(a.warm[2]));
    }
  }
  TSTAMP(7);
  const unsigned kh = w >> 2, r16 = lane & 15u, g4 = lane >> 4, q4 = r16 >> 2, p = lane & 3u;
  const unsigned g = h >> 3, j = h & 7u;
  gu64* const flags = (gu64*)(c.ws + WS_FLAG);
  bar();
  if (!L && w == 0 && lane == 0) __hip_atomic_store(flags + (b * 32u + h) * 16u, c.tag, __ATOMIC_RELAXED, __HIP_MEMORY_SCOPE_AGENT);
  TSTAMP(8);
  unsigned ba[2][2], ao[2];
#pragma unroll
  for (int ks = 0; ks < 2; ++ks) {
    const unsigned ksa = ks ^ (w & 1u);
    ao[ks] = lane * 16u + (2u * kh + ksa) * 1024u;
#pragma unroll
    for (int e = 0; e < 2; ++e) ba[ks][e] = ldsb + (64u * kh + 32u * ksa + 4u * g4 + q4) * 192u + ((((j & 1u) + e) ^ (g4 & 1u)) - e) * 32u + 8u * p;
  }
  bf16x8 Bf[2][2][5], Af[2][2][2], Ar[2];
  auto read_b = [&](int par, int ks, int nt, unsigned slot) { const unsigned ad = ba[ks][nt & 1] + slot + nt * 32; Bf[par][ks][nt] = cat(ldstr(ad), ldstr(ad + 3072)); };
  auto a_off = [&](unsigned s) { return s < 8u ? (g * 8u + ((j + s) & 7u)) * 4096u : 0x40000000u; };
  auto load_a = [&](int par, int ks, unsigned so) {
#pragma unroll
    for (int qt = 0; qt < 2; ++qt) Af[par][ks][qt] = __builtin_bit_cast(bf16x8, __builtin_amdgcn_raw_buffer_load_b128(srdO, ao[ks], (2u * w4 + (qt ^ kh)) * 131072u + so, 16));
    if (!L && ks == 0) Ar[par] = __builtin_bit_cast(bf16x8, __builtin_amdgcn_raw_buffer_load_b128(srdO, ao[0], 8u * 131072u + so, 16));
  };
  load_a(0, 0, a_off(0)); load_a(0, 1, a_off(0));
  GAP();
#pragma unroll
  for (int par = 0; par < 2; ++par)
#pragma unroll
    for (int ks = 0; ks < 2; ++ks)
#pragma unroll
      for (int nt = 0; nt < 5; ++nt) read_b(par, ks, nt, bslot(par));
  GAP();
  dma_b(c, 4, w); dma_b(c, 5, w);
  f32x4 acc[2][5], accr[5];
#pragma unroll
  for (int nt = 0; nt < 5; ++nt) { zero(acc[0][nt]); zero(acc[1][nt]); if (!L) zero(accr[nt]); }
  bar();
  dma_b(c, 6, w); dma_b(c, 7, w);
  if (!L && w == 0) {
    gu64* f = flags + (b * 32u + g * 8u + (lane & 7u)) * 16u;
#pragma nounroll
    for (unsigned spin = 0; spin < (1u << 16); ++spin) {
      if (__all(__hip_atomic_load(f, __ATOMIC_RELAXED, __HIP_MEMORY_SCOPE_AGENT) == c.tag)) break;
      __builtin_amdgcn_s_sleep(1);
    }
  }
  TSTAMP(9);
  waitvm<0>();
  bar();
  TSTAMP(10);
  load_a(1, 0, a_off(1)); load_a(1, 1, a_off(1));
  GAP();
#pragma nounroll
  for (unsigned s = 0; s < 8u; s += 2u) {
#pragma unroll
    for (int par = 0; par < 2; ++par) {
      const unsigned sn = s + par + 2u, so = a_off(sn);
      const unsigned slot = sn == 2u ? 0u : sn == 6u ? bslot(6) : sn >= 7u ? BSZ : (sn - 1u) * BSZ;
#pragma unroll
      for (int ks = 0; ks < 2; ++ks) {
#pragma unroll
        for (int nt = 0; nt < 5; ++nt) {
          mma(acc[0][nt], Bf[par][ks][nt], Af[par][ks][0]);
          mma(acc[1][nt], Bf[par][ks][nt], Af[par][ks][1]);
          if (!L && ks == 0) mma(accr[nt], Bf[par][ks][nt], Ar[par]);
          read_b(par, ks, nt, slot); GAP();
        }
        load_a(par, ks, so); GAP();
      }
    }
  }
  mma_done();
  TSTAMP(11);
  bar();
#pragma unroll
  for (int nt = 0; nt < 5; ++nt) *(LAS f32x4*)(ldsb + w * 5120u + nt * 1024u + lane * 16u) = acc[1][nt];
  if constexpr (!L) {
#pragma unroll
    for (int nt = 0; nt < 5; ++nt) *(LAS f32x4*)(ldsb + 40960u + w4 * 5120u + nt * 1024u + lane * 16u) = accr[nt];
  }
  bar();
  const srd_t srdM = mksrd(c.ws + WS_MBOX + (size_t)b * 32u * MBOX, 32 * MBOX);
  {
    const unsigned vo = q4 * (8u * (unsigned)MBOX) + (g4 * 4u + p) * 16u, so = j * (unsigned)MBOX + g * 11520u;
#pragma unroll
    for (int nt = 0; nt < 5; ++nt) {
      const f32x4 v = acc[0][nt] + *(LAS f32x4*)(ldsb + (w ^ 4u) * 5120u + nt * 1024u + lane * 16u);
      store_wt(__builtin_bit_cast(u32x4, v), srdM, vo, so + ((2u * w4 + kh) * 5u + nt) * 256u);
    }
    if constexpr (L) {
#pragma nounroll
      for (unsigned nt = w4; nt < 5u; nt += 4u) {
        f32x4 v = kZero4;
#pragma unroll
        for (int i = 0; i < 4; ++i) v += *(LAS f32x4*)(ldsb + 40960u + i * 5120u + nt * 1024u + lane * 16u);
        store_wt(__builtin_bit_cast(u32x4, v), srdM, vo, so + (40u + nt) * 256u);
      }
    }
  }
  TSTAMP(12);
  waitvm<0>();
  bar();
  if (!L && w == 0) {
    if (lane == 0) __hip_atomic_store(flags + (256u + b * 32u + h) * 16u, c.tag, __ATOMIC_RELAXED, __HIP_MEMORY_SCOPE_AGENT);
    TSTAMP(13);
    gu64* f = flags + (256u + b * 32u + (lane & 3u) * 8u + j) * 16u;
#pragma nounroll
    for (unsigned spin = 0; spin < (1u << 16); ++spin) {
      if (__all(__hip_atomic_load(f, __ATOMIC_RELAXED, __HIP_MEMORY_SCOPE_AGENT) == c.tag)) break;
      __builtin_amdgcn_s_sleep(1);
    }
    if (h == 0 && lane == 0) __hip_atomic_store(flags + (512u + b) * 16u, c.tag, __ATOMIC_RELAXED, __HIP_MEMORY_SCOPE_AGENT);
  }
  bar();
  TSTAMP(14);
  {
    f32x4 v[2][4];
#pragma unroll
    for (int k = 0; k < 2; ++k)
#pragma unroll
      for (int i = 0; i < 4; ++i) {
        const unsigned u = w * 64u + lane + 512u * k;
        v[k][i] = __builtin_bit_cast(f32x4, __builtin_amdgcn_raw_buffer_load_b128(srdM, u < 720u ? u * 16u : 0x40000000u, h * (unsigned)MBOX + i * 11520u, 16));
      }
#pragma unroll
    for (int k = 0; k < 2; ++k) {
      const unsigned u = w * 64u + lane + 512u * k;
      const f32x4 r = (v[k][0] + v[k][1]) + (v[k][2] + v[k][3]);
      const unsigned t = u >> 4, mt = t / 5u, nt = t - 5u * mt;
      if (u < 720u) __hip_atomic_store((gu64*)(c.out + (size_t)(b * SQ + 16u * mt + 4u * g + (u & 3u)) * DM + 80u * j + 16u * nt + ((u >> 2) & 3u) * 4u), ((unsigned long long)cvtpk(r[2], r[3]) << 32) | cvtpk(r[0], r[1]), __ATOMIC_RELAXED, __HIP_MEMORY_SCOPE_AGENT);
    }
  }
  STAMP_END;
}

__global__ __launch_bounds__(512, 2) void fused_kernel(const u16* __restrict__ x, const u16* __restrict__ kc, const u16* __restrict__ vc,
                                                       const u16* __restrict__ wq, const u16* __restrict__ wo, char* ws, u16* __restrict__ out) {
  extern __shared__ __attribute__((aligned(16))) char lds[];
  unsigned long long pc; asm volatile("s_getpc_b64 %0" : "=s"(pc));
  Ctx c;
  STAMP(0);
  const unsigned ldsb = (unsigned)(size_t)(LAS char*)lds;
  const unsigned lane = threadIdx.x & 63u;
  const unsigned w = rfl(threadIdx.x >> 6);
  const unsigned bid = blockIdx.x, xg = bid & 7u, wi = bid >> 3;
  const unsigned h = (xg & 3u) * 8u + (wi & 7u), b = (xg >> 2) * 4u + (wi >> 3);
  const unsigned w4 = w & 3u;
  const bool loader = ((w + 2u) & 4u) != 0u;
  const unsigned r16 = lane & 15u, g4 = lane >> 4, q4 = r16 >> 2, p = lane & 3u;
  const unsigned wsw = (q4 << 2) | ((g4 & 1u) << 1);
  c.ldsb = ldsb; c.w = w; c.w4 = w4; c.b = b; c.h = h; c.wi = wi; c.ws = ws; c.out = out; c.wo = wo; c.tag = 0;
  c.srdB = mksrd((const char*)wo + (size_t)(h >> 3) * 1024u * DM * 2u + (h & 7u) * 160u - (h & 1u) * 32u, 1023 * DM * 2 + 192);

  if (loader) {
    const srd_t srdX = mksrd(x + (size_t)b * SQ * DM, SQ * DM * 2);
    const srd_t srdW = mksrd(wq + h * DH, (DM * HD - h * DH) * 2);
    unsigned xoff[4], woff[4];
#pragma unroll
    for (int i = 0; i < 4; ++i) {
      xoff[i] = (32u * w4 + 8u * i + (lane >> 3)) * (DM * 2u) + (((lane & 7u) ^ (4u * (i & 1) + g4)) << 4) - i * 1024u;
      woff[i] = (16u * w4 + 4u * i + g4) * (HD * 2u) + ((r16 ^ ((g4 << 2) | (i & 2))) << 4) - i * 1024u;
    }
    const unsigned xoff4 = (128u + 4u * w4 + (lane >> 3)) * (DM * 2u) + (((lane & 7u) ^ ((2u * w4 + g4) & 7u)) << 4);
    unsigned kk = ROT;
    auto dma_stage = [&](unsigned wsl, unsigned xsl) {
      dma4<false>(ldsb + WS0 + wsl + w4 * 4096u, woff, srdW, kk * (64u * HD * 2u));
      dma4<false>(ldsb + XS0 + xsl + w4 * 4096u, xoff, srdX, kk * 128u);
      dma_half(ldsb + XS0 + xsl + 16384u + w4 * 512u, xoff4, srdX, kk * 128u);
      kk = kk == 9u ? 0u : kk + 1u;
    };
    QSTAMP(7);
    dma_stage(2u * SLOT, 0u);
    QSTAMP(8);
    dma_stage(0u, XSZ);
    QSTAMP(9);
    c.srdK = mksrd(kc + (size_t)(b * NH + h) * SK * DH, SK * DH * 2);
    c.srdV = mksrd(vc + (size_t)(b * NH + h) * SK * DH, SK * DH * 2);
#pragma unroll
    for (int i = 0; i < 4; ++i) {
      c.koff[i] = (16u * w4 + g4) * 256u + ((r16 ^ (4u * i + g4)) << 4);
      c.voff[i] = (16u * w4 + g4) * 256u + ((r16 ^ ((g4 << 2) | ((i & 1) << 1))) << 4);
    }
    f32x4 acc[2] = {kZero4, kZero4};
    unsigned xa[2], wa[2];
#pragma unroll
    for (int s = 0; s < 2; ++s) xa[s] = ldsb + XS0 + (128u + r16) * 128u + (((4u * s + g4) ^ (r16 >> 1)) << 4);
#pragma unroll
    for (int e = 0; e < 2; ++e) wa[e] = ldsb + WS0 + (8u * g4 + q4) * 256u + (((4u * w4 + 2u * e + (p >> 1)) ^ wsw) << 4) + 8u * (p & 1u);
    bf16x8 xf[2], wf[2][2];
    xf[0] = xf[1] = wf[0][0] = wf[0][1] = wf[1][0] = wf[1][1] = bf16x8{};
    auto mm = [&]() {
#pragma unroll
      for (int s = 0; s < 2; ++s)
#pragma unroll
        for (int e = 0; e < 2; ++e) acc[e] = __builtin_amdgcn_mfma_f32_16x16x32_bf16(wf[s][e], xf[s], acc[e], 0, 0, 0);
      GAP();
    };
    auto stage = [&](unsigned wsl, unsigned xsl, unsigned wprev, unsigned xprev, int kind) {
      if (kind == 2) waitvm<4>(); else waitvm<9>();
      bar();
      mm();
#pragma unroll
      for (int s = 0; s < 2; ++s) {
        xf[s] = lds128(xa[s] + xsl);
#pragma unroll
        for (int e = 0; e < 2; ++e) { const unsigned ad = wa[e] + wsl + s * 8192; wf[s][e] = cat(ldstr(ad), ldstr(ad + 1024)); }
      }
      GAP();
      if (kind == 0) dma_stage(wprev, xprev); else dma_k(c, kind - 1);
      GAP();
    };
    __builtin_amdgcn_s_setprio(1);
    stage(2u * SLOT, 0u, SLOT, 2u * XSZ, 0);
    QSTAMP(12);
    stage(0u, XSZ, 2u * SLOT, 0u, 0);
#pragma nounroll
    for (unsigned it = 0; it < 2u; ++it) {
      stage(SLOT, 2u * XSZ, 0u, XSZ, 0);
      stage(2u * SLOT, 0u, SLOT, 2u * XSZ, 0);
      if (it == 0) QSTAMP(13); else QSTAMP(14);
      stage(0u, XSZ, 2u * SLOT, 0u, 0);
    }
    stage(SLOT, 2u * XSZ, 0u, 0u, 1);
    stage(2u * SLOT, 0u, 0u, 0u, 2);
    bar();
    mm();
    __builtin_amdgcn_s_setprio(0);
#pragma unroll
    for (int e = 0; e < 2; ++e) {
      u32x2 q = {cvtpk(acc[e][0] * QSCALE, acc[e][1] * QSCALE), cvtpk(acc[e][2] * QSCALE, acc[e][3] * QSCALE)};
      *(LAS u32x2*)(ldsb + QI + 8u * 4096u + w4 * 1024u + ((2u * e + (g4 >> 1)) * 16u + r16) * 16u + (g4 & 1u) * 8u) = q;
    }
    STAMP(1);
    waitvm<4>();
    bar();
    dma_v(c, 0);
    STAMP(2);
    attend_project<4>(c, lane);
  } else {
    if (w == 0) {
      const unsigned long long e = __hip_atomic_load((gu64*)(ws + WS_FLAG) + (512u + b) * 16u, __ATOMIC_RELAXED, __HIP_MEMORY_SCOPE_AGENT) + 1ull;
      c.tag = ((unsigned long long)rfl((unsigned)(e >> 32)) << 32) | rfl((unsigned)e);
    }
    if (w == 1) {
      const srd_t srdC = mksrd((const void*)(pc & ~255ull), CODE_LINES * 128);
      unsigned ln = wi * (CODE_LINES / 32) + (lane >> 1); ln = ln >= CODE_LINES ? ln - CODE_LINES : ln;
      unsigned dummy;
      asm volatile("s_nop 4\n\tbuffer_load_dword %0, %1, %2, 0 offen\n\ts_waitcnt vmcnt(0)" : "=&v"(dummy) : "v"(ln * 128u + (lane & 1u) * 64u), "s"(srdC) : "memory");
    }
    const unsigned qh = w4 & 1u, dh = w4 >> 1;
    f32x4 acc[4][4];
#pragma unroll
    for (int dt = 0; dt < 4; ++dt)
#pragma unroll
      for (int qt = 0; qt < 4; ++qt) zero(acc[dt][qt]);
    unsigned xa[2], wa[4];
#pragma unroll
    for (int s = 0; s < 2; ++s) xa[s] = ldsb + XS0 + (64u * qh + r16) * 128u + (((4u * s + g4) ^ (r16 >> 1)) << 4);
#pragma unroll
    for (int dt = 0; dt < 4; ++dt) wa[dt] = ldsb + WS0 + 2u * SLOT + (8u * g4 + q4) * 256u + (((8u * dh + 2u * dt + (p >> 1)) ^ wsw) << 4) + 8u * (p & 1u);
    bf16x8 xf[2][4];
    bf16x4 wl[2][4], wu[2][4];
    auto rd = [&](int s, int r) {
      if (r == 0) wl[s][0] = ldstr(wa[0] + s * 8192);
      else if (r == 1) wu[s][0] = ldstr(wa[0] + s * 8192 + 1024);
      else if (r < 6) xf[s][r - 2] = lds128(xa[s] + (r - 2) * 2048);
      else if (r & 1) wu[s][(r - 4) >> 1] = ldstr(wa[(r - 4) >> 1] + s * 8192 + 1024);
      else wl[s][(r - 4) >> 1] = ldstr(wa[(r - 4) >> 1] + s * 8192);
      GAP();
    };
    bar();
#pragma unroll
    for (int r = 0; r < 12; ++r) rd(0, r);
    unsigned sl = 0, dx = XSZ, dw = -2 * (int)SLOT;
#pragma nounroll
    for (unsigned sg = 0; sg < 10u; ++sg) {
#pragma unroll
      for (int m = 0; m < 16; ++m) {
        mma(acc[m >> 2][m & 3], cat(wl[0][m >> 2], wu[0][m >> 2]), xf[0][m & 3]);
        if (m < 12) rd(1, m);
        if (m == 12) { xa[0] += dx; xa[1] += dx; GAP(); }
        if (m == 13) { wa[0] += dw; wa[1] += dw; GAP(); }
        if (m == 14) { wa[2] += dw; wa[3] += dw; GAP(); }
      }
#pragma unroll
      for (int m = 0; m < 16; ++m) {
        mma(acc[m >> 2][m & 3], cat(wl[1][m >> 2], wu[1][m >> 2]), xf[1][m & 3]);
        if (m == 0) bar();
        if (m < 12) rd(0, m);
        if (m == 13) { sl = sl == 2u ? 0u : sl + 1u; asm volatile("" : "+s"(sl)); GAP(); }
        if (m == 14) { dx = sl == 2u ? -2 * (int)XSZ : XSZ; asm volatile("" : "+s"(dx)); GAP(); }
        if (m == 15) { dw = sl == 0u ? -2 * (int)SLOT : SLOT; asm volatile("" : "+s"(dw)); GAP(); }
      }
    }
    mma_done();
#pragma unroll
    for (int dt = 0; dt < 4; ++dt)
#pragma unroll
      for (int qt = 0; qt < 4; ++qt) {
        u32x2 q = {cvtpk(acc[dt][qt][0] * QSCALE, acc[dt][qt][1] * QSCALE), cvtpk(acc[dt][qt][2] * QSCALE, acc[dt][qt][3] * QSCALE)};
        *(LAS u32x2*)(ldsb + QI + (4u * qh + qt) * 4096u + (2u * dh + (dt >> 1)) * 1024u + ((2u * (dt & 1) + (g4 >> 1)) * 16u + r16) * 16u + (g4 & 1u) * 8u) = q;
      }
    STAMP(1);
    bar();
    STAMP(2);
    attend_project<5>(c, lane);
  }
}

extern "C" void kernel_launch(void* const* d_in, const int* in_sizes, int n_in, void* d_out, int out_size, void* d_ws, size_t ws_size, hipStream_t stream) {
  (void)hipFuncSetAttribute((const void*)fused_kernel, hipFuncAttributeMaxDynamicSharedMemorySize, LDS_A);
  fused_kernel<<<256, 512, LDS_A, stream>>>((const u16*)d_in[0], (const u16*)d_in[1], (const u16*)d_in[2], (const u16*)d_in[3], (const u16*)d_in[4], (char*)d_ws, (u16*)d_out);
}
```

```cpp
#include <hip/hip_runtime.h>
#include <hip/hip_bf16.h>
#include <utility>

typedef unsigned short u16;
typedef float f32x4 __attribute__((ext_vector_type(4)));
typedef float f32x2 __attribute__((ext_vector_type(2)));
typedef __bf16 bf16x8 __attribute__((ext_vector_type(8)));
typedef __bf16 bf16x4 __attribute__((ext_vector_type(4)));
typedef __bf16 bf16x2 __attribute__((ext_vector_type(2)));
typedef unsigned u32x4 __attribute__((ext_vector_type(4)));
typedef unsigned u32x2 __attribute__((ext_vector_type(2)));
typedef __attribute__((address_space(1))) unsigned long long gu64;
typedef __amdgpu_buffer_rsrc_t srd_t;
#define LAS __attribute__((address_space(3)))
#define DI __device__ __forceinline__
#define GAP() __builtin_amdgcn_sched_barrier(0)

constexpr int SQ = 144, DM = 640, NH = 32, SK = 1024, DH = 128;
constexpr int HD = NH * DH;
constexpr float QSCALE = 0.12751743082f;
#define POLLGAP 6
constexpr unsigned SPIN_MAX = 1u << 16;
constexpr unsigned CODE_LINES = 224;

#define STAMPS 0

constexpr unsigned SLOT = 16384;
constexpr unsigned KR = 0, VR = 49152;
constexpr unsigned WS0 = 32768;
constexpr unsigned XS0 = 98304, XSZ = 18432;
constexpr unsigned QI = XS0 + XSZ;
constexpr unsigned PB = 98304;
constexpr unsigned STATS = 159744;
constexpr unsigned TAGW = 163832;
constexpr unsigned BSZ = 40960;
constexpr unsigned OWN = 3 * BSZ;
constexpr unsigned LDS_A = 163840;
constexpr size_t WS_MBOX = 9437184, MBOX = 49152;
constexpr size_t WS_FLAG = WS_MBOX + 256 * MBOX;

DI unsigned rfl(unsigned x) { return __builtin_amdgcn_readfirstlane(x); }
DI unsigned cvtpk(float lo, float hi) { f32x2 v = {lo, hi}; bf16x2 b = __builtin_convertvector(v, bf16x2); return __builtin_bit_cast(unsigned, b); }
DI bf16x8 lds128(unsigned a) { return *(LAS bf16x8*)a; }
DI bf16x4 ldstr(unsigned a) { return __builtin_amdgcn_ds_read_tr16_b64_v4bf16((LAS bf16x4*)a); }
DI bf16x8 cat(bf16x4 a, bf16x4 b) { return __builtin_shufflevector(a, b, 0, 1, 2, 3, 4, 5, 6, 7); }
DI srd_t mksrd(const void* p, unsigned bytes) { return __builtin_amdgcn_make_buffer_rsrc((void*)p, (short)0, bytes, 0x00020000); }
template <int N> DI void waitvm() { asm volatile("s_waitcnt vmcnt(%0)" ::"i"(N) : "memory"); }
DI void bar() { asm volatile("s_waitcnt lgkmcnt(0)\n\ts_barrier" ::: "memory"); GAP(); }
DI void dma(unsigned m0, unsigned voff, srd_t srd, unsigned soff) {
  asm volatile("s_mov_b32 m0, %0\n\ts_nop 0\n\tbuffer_load_dwordx4 %1, %2, %3 offen lds" ::"s"(m0), "v"(voff), "s"(srd), "s"(soff) : "memory");
}
DI void dma_nt(unsigned m0, unsigned voff, srd_t srd, unsigned soff) {
  asm volatile("s_mov_b32 m0, %0\n\ts_nop 0\n\tbuffer_load_dwordx4 %1, %2, %3 offen nt lds" ::"s"(m0), "v"(voff), "s"(srd), "s"(soff) : "memory");
}
DI void set_m0(unsigned m0) { asm volatile("s_mov_b32 m0, %0\n\ts_nop 0" ::"s"(m0) : "memory"); }
template <int OFF, bool NT> DI void dma_o(unsigned voff, srd_t srd, unsigned soff) {
  if constexpr (NT) asm volatile("buffer_load_dwordx4 %0, %1, %2 offen offset:%3 nt lds" ::"v"(voff), "s"(srd), "s"(soff), "i"(OFF) : "memory");
  else asm volatile("buffer_load_dwordx4 %0, %1, %2 offen offset:%3 lds" ::"v"(voff), "s"(srd), "s"(soff), "i"(OFF) : "memory");
}
template <bool NT> DI void dma4(unsigned m0, const unsigned (&voff)[4], srd_t srd, unsigned soff) {
  set_m0(m0); dma_o<0, NT>(voff[0], srd, soff); dma_o<1024, NT>(voff[1], srd, soff); dma_o<2048, NT>(voff[2], srd, soff); dma_o<3072, NT>(voff[3], srd, soff);
}
DI void dma_half(unsigned m0, unsigned voff, srd_t srd, unsigned soff) {
  asm volatile("s_mov_b32 m0, %0\n\ts_mov_b32 exec_hi, 0\n\tbuffer_load_dwordx4 %1, %2, %3 offen lds\n\ts_mov_b32 exec_hi, -1" ::"s"(m0), "v"(voff), "s"(srd), "s"(soff) : "memory");
}
DI void store_wt(u32x4 v, srd_t srd, unsigned voff, unsigned soff) { __builtin_amdgcn_raw_buffer_store_b128(v, srd, voff, soff, 16); GAP(); asm volatile("s_nop 1"); GAP(); }
DI u32x4 load_wt(srd_t srd, unsigned voff, unsigned soff) { return __builtin_amdgcn_raw_buffer_load_b128(srd, voff, soff, 16); }
DI unsigned long long flag_get(gu64* f) { return __hip_atomic_load(f, __ATOMIC_RELAXED, __HIP_MEMORY_SCOPE_AGENT); }
DI void flag_set(gu64* f, unsigned long long v) { __hip_atomic_store(f, v, __ATOMIC_RELAXED, __HIP_MEMORY_SCOPE_AGENT); }
struct Poll {
  unsigned long long v0, v1, v2;
  DI void wait(gu64* f, unsigned long long tag, unsigned off) {
    unsigned n = SPIN_MAX;
    asm volatile("s_cmp_lg_u32 %6, 0\n\ts_cbranch_scc1 2f\n\t"
                 "global_load_dwordx2 %0, %4, off sc1\n\ts_sleep %7\n\tglobal_load_dwordx2 %1, %4, off sc1\n\ts_sleep %7\n\tglobal_load_dwordx2 %2, %4, off sc1\n"
                 "1:\n\t"
                 "s_waitcnt vmcnt(2)\n\tv_cmp_ne_u64 vcc, %5, %0\n\ts_cbranch_vccz 2f\n\tglobal_load_dwordx2 %0, %4, off sc1\n\t"
                 "s_waitcnt vmcnt(2)\n\tv_cmp_ne_u64 vcc, %5, %1\n\ts_cbranch_vccz 2f\n\tglobal_load_dwordx2 %1, %4, off sc1\n\t"
                 "s_waitcnt vmcnt(2)\n\tv_cmp_ne_u64 vcc, %5, %2\n\ts_cbranch_vccz 2f\n\tglobal_load_dwordx2 %2, %4, off sc1\n\t"
                 "s_sub_u32 %3, %3, 1\n\ts_cbranch_scc0 1b\n"
                 "2:"
                 : "=&v"(v0), "=&v"(v1), "=&v"(v2), "+s"(n) : "v"(f), "s"(tag), "s"(off), "i"(POLLGAP) : "vcc", "scc", "memory");
  }
  DI void done() { asm volatile("" ::"v"(v0), "v"(v1), "v"(v2)); }
};
template <int C> DI float dpp_add(float x) { return x + __uint_as_float(__builtin_amdgcn_update_dpp(0u, __float_as_uint(x), C, 0xf, 0xf, true)); }
DI float swap32_add(float x) { auto r = __builtin_amdgcn_permlane32_swap(__float_as_uint(x), __float_as_uint(x), false, false); return __uint_as_float(r[0]) + __uint_as_float(r[1]); }
DI float swap16_add(float x) { auto r = __builtin_amdgcn_permlane16_swap(__float_as_uint(x), __float_as_uint(x), false, false); return __uint_as_float(r[0]) + __uint_as_float(r[1]); }
DI float fadd(float a, float b) { float r; asm("v_add_f32_e32 %0, %1, %2" : "=v"(r) : "v"(a), "v"(b)); return r; }
DI unsigned fresh_lane() { unsigned l; asm volatile("v_mbcnt_lo_u32_b32 %0, -1, 0\n\tv_mbcnt_hi_u32_b32 %0, -1, %0" : "=v"(l)); return l; }
DI void mma(f32x4& d, bf16x8 a, bf16x8 b) { asm volatile("v_mfma_f32_16x16x32_bf16 %0, %1, %2, %0" : "+v"(d) : "v"(a), "v"(b)); GAP(); }
DI void mma0(f32x4& d, bf16x8 a, bf16x8 b) { asm volatile("v_mfma_f32_16x16x32_bf16 %0, %1, %2, 0" : "=&v"(d) : "v"(a), "v"(b)); GAP(); }
DI void mma_done() { GAP(); asm volatile("s_nop 7"); GAP(); }
const f32x4 kZero4 = {0.f, 0.f, 0.f, 0.f};
DI void zero(f32x4& d) { d = kZero4; asm volatile("" : "+v"(d)); }

#define STAMP(i) do {} while (0)
#define STAMP_END do {} while (0)

#define QSTAMP(i) do {} while (0)
#define TSTAMP(i) STAMP(i)
#define OSTAMP(i) do {} while (0)
#define TOUCH 1
#define KG0 4
#define KGS 2
#define VG0 16
#define VGS 4
#define PACE 3
struct Ctx {
  unsigned ldsb, w, w4, b, h, wi;
  unsigned long long tag;
  char* ws; u16* out; const u16* wo;
  srd_t srdK, srdV, srdB;
  unsigned koff[4], voff[4], boff[5];
};
DI void dma_k(const Ctx& c, unsigned t) { dma4<true>(c.ldsb + KR + (t % 3u) * SLOT + c.w4 * 4096u, c.koff, c.srdK, t * SLOT); }
DI void dma_v(const Ctx& c, unsigned t) { dma4<true>(c.ldsb + VR + ((t + 2u) % 3u) * SLOT + c.w4 * 4096u, c.voff, c.srdV, t * SLOT); }
DI void set_boff(Ctx& c, unsigned lane) {
#pragma unroll
  for (int i = 0; i < 5; ++i) { const unsigned ci = 64u * i + lane, row = ci / 20u; c.boff[i] = row * (DM * 2u) + (((ci - 20u * row) ^ (((row >> 2) & 1u) << 1)) << 4) - (i & 3) * 1024u; }
}
template <int S, int I> DI void dma_b1(const Ctx& c) {
  if constexpr (I % 5 == 0 || I % 5 == 4) set_m0(c.ldsb + S * BSZ + c.w4 * 10240u + I * 1024u);
  dma_o<((I % 5) & 3) * 1024, false>(c.boff[I % 5], c.srdB, ((((c.h + S) & 3u) << 7) + c.w4 * 32u + (I / 5) * 16u) * (DM * 2u));
  GAP();
}
template <int S, int... Is> DI void dma_b_(const Ctx& c, std::integer_sequence<int, Is...>) { (dma_b1<S, Is>(c), ...); }
template <int S> DI void dma_b(const Ctx& c) { dma_b_<S>(c, std::make_integer_sequence<int, 10>{}); }
template <int S, int... Is> DI void dma_bp_(const Ctx& c, std::integer_sequence<int, Is...>) { ((waitvm<PACE>(), dma_b1<S, Is>(c)), ...); }
template <int S> DI void dma_bp(const Ctx& c) { dma_bp_<S>(c, std::make_integer_sequence<int, 10>{}); }

template <int NQ> struct Att {
  f32x4 O[NQ][2], S[NQ];
  bf16x8 Q[NQ][4], K[4], V[2][2], P[NQ][2];
  u32x2 pk;
  float l[NQ];
  unsigned ka[4], va[2], pw, pr;
  unsigned rs, m0k, m0v;
  unsigned warm, wv;
};
template <int NQ> constexpr bool blk_pv(int b) { return b >= 2 * NQ - 3 ? true : (b >= 3 && (b & 1)); }
template <int NQ> constexpr int blk_tile(int b) { return b >= 2 * NQ - 3 ? b - NQ : b < 3 ? b : (b & 1) ? (b - 3) / 2 : (b + 2) / 2; }
template <int NQ> constexpr int pv_start(int t) { for (int b = 0; b < 2 * NQ; ++b) if (blk_pv<NQ>(b) && blk_tile<NQ>(b) == t) return 4 * b; return -1; }
template <int NQ, int T, int F, unsigned BUF> DI void sm_op(Att<NQ>& a) {
  if constexpr (F == 0 || F == 1) a.S[T][F] = __builtin_amdgcn_exp2f(a.S[T][F]);
  if constexpr (F == 3 || F == 4) a.S[T][F - 1] = __builtin_amdgcn_exp2f(a.S[T][F - 1]);
  if constexpr (F == 2) { a.l[T] = fadd(a.l[T], a.S[T][0]); GAP(); a.pk[0] = cvtpk(a.S[T][0], a.S[T][1]); }
  if constexpr (F == 5) { a.l[T] = fadd(a.l[T], a.S[T][1]); GAP(); a.pk[1] = cvtpk(a.S[T][2], a.S[T][3]); }
  if constexpr (F == 6) { *(LAS u32x2*)(a.pw + BUF + T * 2048) = a.pk; GAP(); a.l[T] = fadd(a.l[T], a.S[T][2]); GAP(); a.l[T] = fadd(a.l[T], a.S[T][3]); }
  GAP();
}
template <int NQ, int PAR, int MODE, int N> DI void gap(Ctx& c, Att<NQ>& a, unsigned i) {
  constexpr int G = 8 * NQ, B = N >> 2, X = N & 3, T = blk_tile<NQ>(B);
  constexpr unsigned CUR = PAR ? 0u : XSZ, NXT = PAR ? XSZ : 0u;
  if constexpr (blk_pv<NQ>(B)) mma(a.O[T][X >> 1], a.V[X & 1][X >> 1], a.P[T][X & 1]);
  else if constexpr (X == 0) mma0(a.S[T], a.K[0], a.Q[T][0]);
  else mma(a.S[T], a.K[X], a.Q[T][X]);
  if constexpr (N == 3) __builtin_amdgcn_s_setprio(3);
  if constexpr (N == 3 + G / 4) __builtin_amdgcn_s_setprio(2);
  if constexpr (N == 3 + G / 2) __builtin_amdgcn_s_setprio(1);
  if constexpr (N == 3 + 3 * G / 4) __builtin_amdgcn_s_setprio(0);
  if constexpr (N + G - 6 < 7 * NQ) sm_op<NQ, NQ - 1, N + G - 6 - 7 * (NQ - 1), CUR>(a);
  if constexpr (N == 2) {
    asm volatile("" : "+s"(a.rs));
    if constexpr (NQ == 4) { a.m0k = a.m0v - (VR - KR); a.m0v = c.ldsb + VR + c.w4 * 4096u + a.rs; }
    const unsigned d = a.rs == 2u * SLOT ? -2 * (int)SLOT : SLOT;
    a.rs += d;
#pragma unroll
    for (int s = 0; s < 4; ++s) a.ka[s] += d;
    a.va[0] += d; a.va[1] += d;
    GAP();
  }
  if constexpr (N == 3) {
    if constexpr (NQ == 4) waitvm<MODE <= 13 ? 4 : 5>();
    bar();
  }
  if constexpr (N >= 3 && N <= 6) { constexpr int f = N - 3; const unsigned ad = a.va[f >> 1] + (f & 1) * 8192; a.V[f & 1][f >> 1] = cat(ldstr(ad), ldstr(ad + 4096)); GAP(); }
  if constexpr (N >= 6) sm_op<NQ, (N - 6) / 7, (N - 6) % 7, NXT>(a);
#pragma unroll
  for (int t = 0; t < NQ; ++t)
#pragma unroll
    for (int ks = 0; ks < 2; ++ks)
      if (N == pv_start<NQ>(t) - (t == 0 ? 5 : 6) + ks) { a.P[t][ks] = lds128(a.pr + CUR + t * 2048 + ks * 1024); GAP(); }
  if constexpr (B == 2 * NQ - 4 && MODE != 14) { a.K[X] = lds128(a.ka[X]); GAP(); }
  if constexpr (NQ == 4) {
    if constexpr (N >= KG0 && N < KG0 + 4 * KGS && (N - KG0) % KGS == 0) {
      constexpr int j = (N - KG0) / KGS;
      if constexpr (MODE <= 12) { if (j == 0) set_m0(a.m0k); dma_o<j * 1024, true>(c.koff[j], c.srdK, (i + 3u) * SLOT); GAP(); }
      if constexpr (MODE == 13) { if (j == 0) set_m0(a.m0v); dma_o<j * 1024, true>(c.voff[j], c.srdV, (i + 2u) * SLOT); GAP(); }
    }
    if constexpr (MODE <= 12 && N >= VG0 && N < VG0 + 4 * VGS && (N - VG0) % VGS == 0) { constexpr int j = (N - VG0) / VGS; if (j == 0) set_m0(a.m0v); dma_o<j * 1024, true>(c.voff[j], c.srdV, (i + 2u) * SLOT); GAP(); }
    if constexpr (MODE == 13 && N == 17) {
      const srd_t s = mksrd((const char*)c.wo + ((size_t)(c.h >> 2) * 512u + c.wi * 16u) * (DM * 2u), 16 * DM * 2);
      asm volatile("buffer_load_dword %0, %1, %2, 0 offen" : "=&v"(a.warm) : "v"(a.wv), "s"(s) : "memory");
      GAP();
    }
    if constexpr (MODE == 14 && N >= 12 && N < 32 && !(N & 1)) dma_b1<0, (N - 12) / 2>(c);
  }
}
template <int NQ, int PAR, int MODE, int... Ns> DI void step_(Ctx& c, Att<NQ>& a, unsigned i, std::integer_sequence<int, Ns...>) { (gap<NQ, PAR, MODE, Ns>(c, a, i), ...); }
template <int NQ, int PAR, int MODE> DI void step(Ctx& c, Att<NQ>& a, unsigned i) { step_<NQ, PAR, MODE>(c, a, i, std::make_integer_sequence<int, 8 * NQ>{}); }
template <int NQ, unsigned BUF, int LO, int... Ns> DI void sm_run(Att<NQ>& a, std::integer_sequence<int, Ns...>) { ((sm_op<NQ, (LO + Ns) / 7, (LO + Ns) % 7, BUF>(a)), ...); }

template <int NQ> DI void attend_project(Ctx& c, unsigned lane) {
  constexpr bool L = NQ == 4;
  const unsigned ldsb = c.ldsb, w = c.w, w4 = c.w4, h = c.h, b = c.b;
  constexpr unsigned T0 = L ? 5u : 0u;
  const srd_t srdO = mksrd(c.ws + (size_t)b * 9u * 131072u, 9 * 131072);
  {
    Att<NQ> a;
    {
      const unsigned r16 = lane & 15u, g4 = lane >> 4, q4 = r16 >> 2, p = lane & 3u;
#pragma unroll
      for (int s = 0; s < 4; ++s) a.ka[s] = ldsb + KR + (16u * w4 + r16) * 256u + (((4u * s + g4) ^ r16) << 4);
#pragma unroll
      for (int e = 0; e < 2; ++e) a.va[e] = ldsb + VR + SLOT + (4u * g4 + q4) * 256u + (((4u * w4 + 2u * e + (p >> 1)) ^ ((q4 << 2) | ((g4 & 1u) << 1))) << 4) + 8u * (p & 1u);
      a.pr = ldsb + PB + T0 * 2048u + lane * 16u;
      a.pw = a.pr + (w4 >> 1) * 1024u + (w4 & 1u) * 8u;
    }
#pragma unroll
    for (int s = 0; s < 4; ++s) a.K[s] = lds128(a.ka[s]);
#pragma unroll
    for (int t = 0; t < NQ; ++t)
#pragma unroll
      for (int s = 0; s < 4; ++s) a.Q[t][s] = lds128(ldsb + QI + (T0 + t) * 4096u + s * 1024u + lane * 16u);
    { const u32x2 t = *(LAS u32x2*)(ldsb + TAGW); c.tag = ((unsigned long long)rfl(t[1]) << 32) | rfl(t[0]); }
    if constexpr (L) waitvm<12>();
    bar();
#pragma unroll
    for (int t = 0; t < NQ; ++t) {
      mma0(a.S[t], a.K[0], a.Q[t][0]);
#pragma unroll
      for (int s = 1; s < 4; ++s) mma(a.S[t], a.K[s], a.Q[t][s]);
    }
#pragma unroll
    for (int s = 0; s < 4; ++s) { a.ka[s] += SLOT; a.K[s] = lds128(a.ka[s]); }
    a.rs = SLOT;
#pragma unroll
    for (int t = 0; t < NQ; ++t) { zero(a.O[t][0]); zero(a.O[t][1]); a.l[t] = 0.f; }
    GAP();
    sm_run<NQ, XSZ, 0>(a, std::make_integer_sequence<int, 8 * NQ - 6>{});
    if constexpr (L) a.m0v = ldsb + VR + w4 * 4096u;
    STAMP(3);
#pragma nounroll
    for (unsigned i = 0; i < (L ? 12u : 14u); i += 2u) {
      step<NQ, 0, 0>(c, a, i);
      step<NQ, 1, 0>(c, a, i + 1u);
    }
    if constexpr (L) {
      { const unsigned l = fresh_lane(); set_boff(c, l); a.wv = l < 40u ? (w4 * 40u + l) * 128u : 0x40000000u; GAP(); }
      step<NQ, 0, 12>(c, a, 12u);
      step<NQ, 1, 13>(c, a, 13u);
    }
    step<NQ, 0, 14>(c, a, 14u);
    asm volatile("s_nop 0");
    sm_run<NQ, 0u, 8 * NQ - 6>(a, std::make_integer_sequence<int, 6 - NQ>{});
    lane = fresh_lane();
    {
      const unsigned sa = ldsb + STATS + T0 * 256u + (lane & 15u) * 16u;
#pragma unroll
      for (int t = 0; t < NQ; ++t) {
        const float l = swap32_add(swap16_add(a.l[t]));
        if (lane < 16u) *(LAS float*)(sa + t * 256 + w4 * 4u) = l;
      }
      if constexpr (L) waitvm<11>();
      bar();
#pragma unroll
      for (int f = 0; f < 4; ++f) { const unsigned ad = a.va[f >> 1] + SLOT + (f & 1) * 8192; a.V[f & 1][f >> 1] = cat(ldstr(ad), ldstr(ad + 4096)); GAP(); if (f == 1) { a.P[0][0] = lds128(a.pr); a.P[0][1] = lds128(a.pr + 1024); GAP(); } }
      f32x4 ls[NQ];
#pragma unroll
      for (int t = 0; t < NQ; ++t) { if (t > 0) { a.P[t][0] = lds128(a.pr + t * 2048); a.P[t][1] = lds128(a.pr + t * 2048 + 1024); } ls[t] = *(LAS f32x4*)(sa + t * 256); GAP(); }
      float inv[NQ];
      auto put = [&](int t) {
        const f32x4 o0 = a.O[t][0] * inv[t], o1 = a.O[t][1] * inv[t];
        const u32x4 f = {cvtpk(o0[0], o0[1]), cvtpk(o0[2], o0[3]), cvtpk(o1[0], o1[1]), cvtpk(o1[2], o1[3])};
        store_wt(f, srdO, lane * 16u, h * 36864u + (T0 + t) * 4096u + w4 * 1024u);
        *(LAS u32x4*)(ldsb + OWN + (T0 + t) * 4096u + w4 * 1024u + lane * 16u) = f; GAP();
      };
#pragma unroll
      for (int t = 0; t < NQ; ++t) {
#pragma unroll
        for (int x = 0; x < 4; ++x) mma(a.O[t][x >> 1], a.V[x & 1][x >> 1], a.P[t][x & 1]);
        inv[t] = __builtin_amdgcn_rcpf((ls[t][0] + ls[t][1]) + (ls[t][2] + ls[t][3])); asm volatile("" : "+v"(inv[t])); GAP();
        if (t >= 2) put(t - 2);
      }
      STAMP(6);
      put(NQ - 2);
      put(NQ - 1);
      if constexpr (L) { waitvm<NQ>(); asm volatile("" ::"v"(a.warm)); }
    }
  }
  const unsigned r16 = lane & 15u, g4 = lane >> 4, q4 = r16 >> 2, p = lane & 3u;
  const unsigned g = h >> 2, j = h & 3u;
  gu64* const flags = (gu64*)(c.ws + WS_FLAG);
  const srd_t srdM = mksrd(c.ws + WS_MBOX + (size_t)b * 32u * MBOX, 32 * MBOX);
  unsigned ba[2];
  ba[0] = ldsb + (4u * g4 + q4) * 320u + 8u * p + (g4 & 1u) * 32u; ba[1] = ba[0] - (g4 & 1u) * 64u;
  auto a_off = [&](unsigned s) { return (g * 4u + ((j + s) & 3u)) * 36864u; };
  auto mail = [&](unsigned U, const f32x4& x, const f32x4& y) {
    store_wt(u32x4{cvtpk(x[0], x[1]), cvtpk(x[2], x[3]), cvtpk(y[0], y[1]), cvtpk(y[2], y[3])}, srdM, lane * 16u, ((U & 7u) * 4u + j) * (unsigned)MBOX + (U >> 3) * 8192u + g * 1024u);
  };
  bf16x8 Bf[10];
  auto next_b = [&]() { ba[0] += BSZ; ba[1] += BSZ; asm volatile("" : "+v"(ba[0]), "+v"(ba[1])); GAP(); };
  auto read_b = [&]() {
#pragma unroll
    for (int nt = 0; nt < 10; ++nt) { const unsigned ad = ba[nt & 1] + nt * 32; Bf[nt] = cat(ldstr(ad), ldstr(ad + 5120)); }
  };
  unsigned back;
  auto touch = [&]() { asm volatile("buffer_load_dword %0, %1, %2, %3 offen sc1" : "=&v"(back) : "v"(lane < 36u ? (w * 36u + lane) * 128u : 0x40000000u), "s"(srdO), "s"(h * 36864u) : "memory"); };
  Poll poll;
  bar();
  TSTAMP(7);
  if constexpr (!L) {
    u32x4 Af[16][2];
#pragma unroll
    for (int ks = 0; ks < 4; ++ks) {
      if (ks == 1) read_b();
#pragma unroll
      for (int m = 0; m < 2; ++m) Af[ks][m] = *(LAS u32x4*)(ldsb + OWN + (2u * w4 + m) * 4096u + ks * 1024u + lane * 16u);
      GAP();
    }
    const unsigned ao = lane * 16u + w4 * 8192u;
    auto load_a = [&](int f) { const int u = f >> 1, m = f & 1; Af[u][m] = load_wt(srdO, ao + (u & 3) * 1024u, a_off(u >> 2) + m * 4096u); GAP(); };
    f32x4 acc[2][10];
#pragma unroll
    for (int u = 0; u < 16; ++u) {
      if (u == 4) {
        waitvm<0>();
        bar();
        if (w == 0 && lane == 0) flag_set(flags + (8u + b * 32u + h) * 16u, c.tag);
        TSTAMP(8);
        touch();
        next_b(); read_b();
        poll.wait(flags + (8u + b * 32u + g * 4u + ((j + 1u + p % 3u) & 3u)) * 16u, c.tag, w);
        bar();
        TSTAMP(9);
        load_a(8); load_a(9); load_a(10); load_a(11);
        TSTAMP(10);
      }
#pragma unroll
      for (int nt = 0; nt < 10; ++nt) {
        const int ru = nt == 0 ? u : u + 1, rn = nt == 0 ? 9 : nt - 1;
        const bool refill = ru != 0 && ru != 4 && ru != 16;
        if ((u == 7 || u == 11) && nt == 1) next_b();
        const unsigned ad = ba[rn & 1] + (ru & 3) * 10240 + rn * 32;
        bf16x4 lo, hi;
        if (u == 0) mma0(acc[0][nt], Bf[nt], __builtin_bit_cast(bf16x8, Af[u][0])); else mma(acc[0][nt], Bf[nt], __builtin_bit_cast(bf16x8, Af[u][0]));
        if ((u == 7 || u == 11) && nt == 1) bar();
        if (u == 4 && nt == 1) { poll.done(); asm volatile("" ::"v"(back)); }
        if (refill) { lo = ldstr(ad); GAP(); }
        if (u == 0) mma0(acc[1][nt], Bf[nt], __builtin_bit_cast(bf16x8, Af[u][1])); else mma(acc[1][nt], Bf[nt], __builtin_bit_cast(bf16x8, Af[u][1]));
        if (refill) { hi = ldstr(ad + 5120); GAP(); Bf[rn] = cat(lo, hi); }
        if (u >= 4 && (nt == 0 || nt == 3 || nt == 6) && 12 + 3 * (u - 4) + nt / 3 < 32) load_a(12 + 3 * (u - 4) + nt / 3);
        if (u == 15 && nt >= 5 && (nt & 1)) { mail(10u * w4 + (nt - 5) / 2, acc[0][nt - 5], acc[0][nt - 4]); mail(10u * w4 + 5u + (nt - 5) / 2, acc[1][nt - 5], acc[1][nt - 4]); }
      }
    }
    TSTAMP(11);
#pragma unroll
    for (int np = 3; np < 5; ++np) { mail(10u * w4 + np, acc[0][2 * np], acc[0][2 * np + 1]); mail(10u * w4 + 5u + np, acc[1][2 * np], acc[1][2 * np + 1]); }
    TSTAMP(12);
  } else {
    u32x4 Af[4][4];
    f32x4 acc[3] = {kZero4, kZero4, kZero4};
    unsigned bl[3] = {ba[0] + 64u * w4, ba[1] + 64u * w4 + 32u, ((w4 & 1u) ? ba[1] + 32u : ba[0]) + 256u};
    auto mm = [&](int s) {
      bf16x8 f[4][3];
#pragma unroll
      for (int ks = 0; ks < 4; ++ks)
#pragma unroll
        for (int i = 0; i < 3; ++i) f[ks][i] = cat(ldstr(bl[i] + ks * 10240), ldstr(bl[i] + ks * 10240 + 5120));
#pragma unroll
      for (int ks = 0; ks < 4; ++ks)
#pragma unroll
        for (int i = 0; i < 3; ++i) acc[i] = __builtin_amdgcn_mfma_f32_16x16x32_bf16(f[ks][i], __builtin_bit_cast(bf16x8, Af[s][ks]), acc[i], 0, 0, 0);
#pragma unroll
      for (int i = 0; i < 3; ++i) { bl[i] += BSZ; asm volatile("" : "+v"(bl[i])); }
      GAP();
    };
    auto load_a = [&](int s) {
#pragma unroll
      for (int ks = 0; ks < 4; ++ks) Af[s][ks] = load_wt(srdO, lane * 16u + ks * 1024u, a_off(s) + 8u * 4096u);
      GAP();
    };
    __builtin_amdgcn_s_setprio(1);
#pragma unroll
    for (int ks = 0; ks < 4; ++ks) Af[0][ks] = *(LAS u32x4*)(ldsb + OWN + 8u * 4096u + ks * 1024u + lane * 16u);
    dma_b<1>(c);
    mm(0);
    OSTAMP(1);
    waitvm<0>();
    bar();
    TSTAMP(8);
    touch();
    bar();
    TSTAMP(9);
    __builtin_amdgcn_s_sleep(2);
    dma_bp<2>(c);
    load_a(1);
    TSTAMP(10);
    waitvm<4>();
    asm volatile("" ::"v"(back));
    OSTAMP(2); bar(); OSTAMP(3);
    dma_bp<3>(c);
    load_a(2); load_a(3);
    mm(1);
    waitvm<8>();
    OSTAMP(4); bar(); OSTAMP(5);
    mm(2);
    mm(3);
    mail(40u + w4, acc[0], acc[1]);
    __builtin_amdgcn_raw_buffer_store_b64(u32x2{cvtpk(acc[2][0], acc[2][1]), cvtpk(acc[2][2], acc[2][3])}, srdM, w4 < 2u ? lane * 16u + w4 * 8u : 0x40000000u, (16u + j) * (unsigned)MBOX + 5u * 8192u + g * 1024u, 16);
  }
  waitvm<0>();
  bar();
  if (L && w4 == 0u && lane == 0) flag_set(flags + (264u + b * 32u + h) * 16u, c.tag);
  TSTAMP(13);
  if constexpr (L) poll.wait(flags + (264u + b * 32u + ((g + 1u + (lane & 7u) % 7u) & 7u) * 4u + j) * 16u, c.tag, w4 ^ 1u);
  bar();
  TSTAMP(14);
  if (L && w4 == 0u && h == 0u && lane == 0) flag_set(flags + b * 16u, c.tag);
  const unsigned k = L ? 4u + (w4 ^ 2u) : w4, U = 8u * k + g;
  if (U < 45u) {
    const unsigned mt = U / 5u, np = U - 5u * mt;
    u32x4 in[8];
#pragma unroll
    for (int i = 0; i < 8; ++i) in[i] = load_wt(srdM, lane * 16u + (i & 3) * 1024u, h * (unsigned)MBOX + k * 8192u + (i >> 2) * 4096u);
    float v[8];
#pragma unroll
    for (int e = 0; e < 8; ++e) {
      float t[8];
#pragma unroll
      for (int i = 0; i < 8; ++i) t[i] = __uint_as_float((e & 1) ? in[i][e >> 1] & 0xffff0000u : in[i][e >> 1] << 16);
      v[e] = ((t[0] + t[1]) + (t[2] + t[3])) + ((t[4] + t[5]) + (t[6] + t[7]));
    }
    u16* const o = c.out + (size_t)(b * SQ + 16u * mt + r16) * DM + 160u * j + 32u * np + 4u * g4;
    *(u32x2*)o = u32x2{cvtpk(v[0], v[1]), cvtpk(v[2], v[3])};
    *(u32x2*)(o + 16) = u32x2{cvtpk(v[4], v[5]), cvtpk(v[6], v[7])};
  }
  if constexpr (L) { waitvm<0>(); poll.done(); }
  STAMP_END;
}

__global__ __launch_bounds__(512, 2) void fused_kernel(const u16* __restrict__ x, const u16* __restrict__ kc, const u16* __restrict__ vc,
                                                       const u16* __restrict__ wq, const u16* __restrict__ wo, char* ws, u16* __restrict__ out) {
  extern __shared__ __attribute__((aligned(16))) char lds[];
  unsigned long long pc; asm volatile("s_getpc_b64 %0" : "=s"(pc));
  Ctx c;
  STAMP(0);
  const unsigned ldsb = (unsigned)(size_t)(LAS char*)lds;
  const unsigned lane = threadIdx.x & 63u;
  const unsigned w = rfl(threadIdx.x >> 6);
  const unsigned bid = blockIdx.x, xg = bid & 7u, wi = bid >> 3;
  const unsigned h = xg * 4u + (wi & 3u), b = wi >> 2;
  const unsigned w4 = w & 3u;
  const bool loader = ((w + 2u) & 4u) != 0u;
  const unsigned r16 = lane & 15u, g4 = lane >> 4, q4 = r16 >> 2, p = lane & 3u;
  const unsigned wsw = (q4 << 2) | ((g4 & 1u) << 1);
  c.ldsb = ldsb; c.w = w; c.w4 = w4; c.b = b; c.h = h; c.wi = wi; c.ws = ws; c.out = out; c.wo = wo; c.tag = 0;
  c.srdB = mksrd((const char*)wo + (size_t)(h >> 2) * 512u * DM * 2u + (h & 3u) * 320u, 511 * DM * 2 + 320);

  if (loader) {
    const srd_t srdX = mksrd(x + (size_t)b * SQ * DM, SQ * DM * 2);
    const srd_t srdW = mksrd(wq + h * DH, (DM * HD - h * DH) * 2);
    unsigned xoff[4], woff[4];
#pragma unroll
    for (int i = 0; i < 4; ++i) {
      xoff[i] = (32u * w4 + 8u * i + (lane >> 3)) * (DM * 2u) + (((lane & 7u) ^ (4u * (i & 1) + g4)) << 4) - i * 1024u;
      woff[i] = (16u * w4 + 4u * i + g4) * (HD * 2u) + ((r16 ^ ((g4 << 2) | (i & 2))) << 4) - i * 1024u;
    }
    const unsigned xoff4 = (128u + 4u * w4 + (lane >> 3)) * (DM * 2u) + (((lane & 7u) ^ ((2u * w4 + g4) & 7u)) << 4);
    auto dma_w = [&](int sg) { dma4<false>(ldsb + WS0 + ((sg + 1) & 3) * SLOT + w4 * 4096u, woff, srdW, sg * (64u * HD * 2u)); };
    auto dma_x = [&](int sg) {
      dma4<false>(ldsb + XS0 + (sg % 3) * XSZ + w4 * 4096u, xoff, srdX, sg * 128u);
      dma_half(ldsb + XS0 + (sg % 3) * XSZ + 16384u + w4 * 512u, xoff4, srdX, sg * 128u);
    };
    QSTAMP(7);
    dma_w(0); dma_x(0); dma_w(1); dma_x(1);
    QSTAMP(9);
    c.srdK = mksrd(kc + (size_t)(b * NH + h) * SK * DH, SK * DH * 2);
    c.srdV = mksrd(vc + (size_t)(b * NH + h) * SK * DH, SK * DH * 2);
#pragma unroll
    for (int i = 0; i < 4; ++i) {
      c.koff[i] = (16u * w4 + g4) * 256u + ((r16 ^ (4u * i + g4)) << 4);
      c.voff[i] = (16u * w4 + g4) * 256u + ((r16 ^ ((g4 << 2) | ((i & 1) << 1))) << 4);
    }
    f32x4 acc[2] = {kZero4, kZero4};
    unsigned xa[2], wa[2];
#pragma unroll
    for (int s = 0; s < 2; ++s) xa[s] = ldsb + XS0 + (128u + r16) * 128u + (((4u * s + g4) ^ (r16 >> 1)) << 4);
#pragma unroll
    for (int e = 0; e < 2; ++e) wa[e] = ldsb + WS0 + (8u * g4 + q4) * 256u + (((4u * w4 + 2u * e + (p >> 1)) ^ wsw) << 4) + 8u * (p & 1u);
    bf16x8 xf[2], wf[2][2];
    auto mm = [&]() {
#pragma unroll
      for (int s = 0; s < 2; ++s)
#pragma unroll
        for (int e = 0; e < 2; ++e) acc[e] = __builtin_amdgcn_mfma_f32_16x16x32_bf16(wf[s][e], xf[s], acc[e], 0, 0, 0);
      GAP();
    };
    __builtin_amdgcn_s_setprio(1);
#pragma unroll
    for (int sg = 0; sg < 10; ++sg) {
      if (sg == 0) waitvm<9>(); else if (sg < 9) waitvm<13>(); else waitvm<8>();
      bar();
      if (sg == 0) QSTAMP(11);
      if (sg > 0) mm();
#pragma unroll
      for (int s = 0; s < 2; ++s) {
        xf[s] = lds128(xa[s] + (sg % 3) * XSZ);
#pragma unroll
        for (int e = 0; e < 2; ++e) { const unsigned ad = wa[e] + ((sg + 1) & 3) * SLOT + s * 8192; wf[s][e] = cat(ldstr(ad), ldstr(ad + 1024)); }
      }
      GAP();
      if (sg == 0) dma_w(2);
      if (sg < 8) dma_x(sg + 2);
      if (sg < 7) dma_w(sg + 3);
      if (sg == 7) dma_k(c, 0);
      if (sg == 8) dma_k(c, 1);
      if (sg == 9) { dma_v(c, 0); dma_k(c, 2); }
      GAP();
      if (sg == 0) QSTAMP(12); else if (sg == 3) QSTAMP(13); else if (sg == 6) QSTAMP(14);
    }
    bar();
    mm();
    __builtin_amdgcn_s_setprio(0);
#pragma unroll
    for (int e = 0; e < 2; ++e) {
      u32x2 q = {cvtpk(acc[e][0] * QSCALE, acc[e][1] * QSCALE), cvtpk(acc[e][2] * QSCALE, acc[e][3] * QSCALE)};
      *(LAS u32x2*)(ldsb + QI + 8u * 4096u + w4 * 1024u + ((2u * e + (g4 >> 1)) * 16u + r16) * 16u + (g4 & 1u) * 8u) = q;
    }
    dma_v(c, 1);
    STAMP(1);
    waitvm<16>();
    bar();
    STAMP(2);
    attend_project<4>(c, lane);
  } else {
    unsigned touched;
    {
      auto tl = [&](const void* base, unsigned bytes, unsigned vo) { asm volatile("s_nop 4\n\tbuffer_load_dword %0, %1, %2, 0 offen" : "=&v"(touched) : "v"(vo), "s"(mksrd(base, bytes)) : "memory"); };
      const unsigned n = h * 36u + lane;
      unsigned ln = wi * (CODE_LINES / 32) + (lane >> 1); ln = ln >= CODE_LINES ? ln - CODE_LINES : ln;
      const unsigned long long pw = (unsigned long long)(wq + h * DH), px = (unsigned long long)(x + (size_t)b * SQ * DM), pcd = pc & ~255ull;
      const unsigned long long base = w4 < 2u ? pw : w4 == 2u ? px : pcd;
      const unsigned bytes = w4 < 2u ? (DM * HD - h * DH) * 2u : w4 == 2u ? SQ * DM * 2u : CODE_LINES * 128u;
      unsigned vo = w4 < 2u ? (128u + (w4 * 32u + (lane >> 1)) * 8u + b) * (HD * 2u) + (lane & 1u) * 128u
                  : w4 == 2u ? (lane < 36u ? (n >> 3) * (DM * 2u) + 256u + (n & 7u) * 128u : 0x40000000u) : ln * 128u + (lane & 1u) * 64u;
      if (!TOUCH && w4 < 3u) vo = 0x40000000u;
      tl((const void*)(((unsigned long long)rfl((unsigned)(base >> 32)) << 32) | rfl((unsigned)base)), rfl(bytes), vo);
    }
    const unsigned long long epoch = flag_get((gu64*)(ws + WS_FLAG) + b * 16u);
    const unsigned qh = w4 & 1u, dh = w4 >> 1;
    f32x4 acc[4][4];
#pragma unroll
    for (int dt = 0; dt < 4; ++dt)
#pragma unroll
      for (int qt = 0; qt < 4; ++qt) zero(acc[dt][qt]);
    unsigned xa[2], wa[4];
#pragma unroll
    for (int s = 0; s < 2; ++s) xa[s] = ldsb + XS0 + (64u * qh + r16) * 128u + (((4u * s + g4) ^ (r16 >> 1)) << 4);
#pragma unroll
    for (int dt = 0; dt < 4; ++dt) wa[dt] = ldsb + WS0 + SLOT + (8u * g4 + q4) * 256u + (((8u * dh + 2u * dt + (p >> 1)) ^ wsw) << 4) + 8u * (p & 1u);
    bf16x8 xf[2][4];
    bf16x4 wl[2][4], wu[2][4];
    auto rd = [&](int s, int r) {
      if (r == 0) wl[s][0] = ldstr(wa[0] + s * 8192);
      else if (r == 1) wu[s][0] = ldstr(wa[0] + s * 8192 + 1024);
      else if (r < 6) xf[s][r - 2] = lds128(xa[s] + (r - 2) * 2048);
      else if (r & 1) wu[s][(r - 4) >> 1] = ldstr(wa[(r - 4) >> 1] + s * 8192 + 1024);
      else wl[s][(r - 4) >> 1] = ldstr(wa[(r - 4) >> 1] + s * 8192);
      GAP();
    };
    bar();
#pragma unroll
    for (int r = 0; r < 12; ++r) rd(0, r);
    unsigned sl = 0, dx = XSZ, dw = SLOT;
#pragma nounroll
    for (unsigned sg = 0; sg < 10u; ++sg) {
#pragma unroll
      for (int m = 0; m < 16; ++m) {
        mma(acc[m >> 2][m & 3], cat(wl[0][m >> 2], wu[0][m >> 2]), xf[0][m & 3]);
        if (m < 12) rd(1, m);
        if (m == 12) { xa[0] += dx; xa[1] += dx; GAP(); }
        if (m == 13) { wa[0] += dw; wa[1] += dw; GAP(); }
        if (m == 14) { wa[2] += dw; wa[3] += dw; GAP(); }
      }
#pragma unroll
      for (int m = 0; m < 16; ++m) {
        mma(acc[m >> 2][m & 3], cat(wl[1][m >> 2], wu[1][m >> 2]), xf[1][m & 3]);
        if (m == 0) bar();
        if (m < 12) rd(0, m);
        if (m == 13) { sl = sl == 2u ? 0u : sl + 1u; asm volatile("" : "+s"(sl)); GAP(); }
        if (m == 14) { dx = sl == 2u ? -2 * (int)XSZ : XSZ; asm volatile("" : "+s"(dx)); GAP(); }
        if (m == 15) { dw = (sg & 3u) == 1u ? -3 * (int)SLOT : SLOT; asm volatile("" : "+s"(dw)); GAP(); }
      }
    }
    mma_done();
#pragma unroll
    for (int dt = 0; dt < 4; ++dt)
#pragma unroll
      for (int qt = 0; qt < 4; ++qt) {
        u32x2 q = {cvtpk(acc[dt][qt][0] * QSCALE, acc[dt][qt][1] * QSCALE), cvtpk(acc[dt][qt][2] * QSCALE, acc[dt][qt][3] * QSCALE)};
        *(LAS u32x2*)(ldsb + QI + (4u * qh + qt) * 4096u + (2u * dh + (dt >> 1)) * 1024u + ((2u * (dt & 1) + (g4 >> 1)) * 16u + r16) * 16u + (g4 & 1u) * 8u) = q;
      }
    STAMP(1);
    waitvm<0>();
    asm volatile("" ::"v"(touched));
    if (w == 0 && lane == 0) *(LAS unsigned long long*)(ldsb + TAGW) = epoch + 1ull;
    bar();
    STAMP(2);
    attend_project<5>(c, lane);
  }
}

extern "C" void kernel_launch(void* const* d_in, const int* in_sizes, int n_in, void* d_out, int out_size, void* d_ws, size_t ws_size, hipStream_t stream) {
  (void)hipFuncSetAttribute((const void*)fused_kernel, hipFuncAttributeMaxDynamicSharedMemorySize, LDS_A);
  fused_kernel<<<256, 512, LDS_A, stream>>>((const u16*)d_in[0], (const u16*)d_in[1], (const u16*)d_in[2], (const u16*)d_in[3], (const u16*)d_in[4], (char*)d_ws, (u16*)d_out);
}
```

```cpp
#include <hip/hip_runtime.h>
#include <hip/hip_bf16.h>
#include <utility>

typedef unsigned short u16;
typedef float f32x4 __attribute__((ext_vector_type(4)));
typedef float f32x2 __attribute__((ext_vector_type(2)));
typedef __bf16 bf16x8 __attribute__((ext_vector_type(8)));
typedef __bf16 bf16x4 __attribute__((ext_vector_type(4)));
typedef __bf16 bf16x2 __attribute__((ext_vector_type(2)));
typedef unsigned u32x4 __attribute__((ext_vector_type(4)));
typedef unsigned u32x2 __attribute__((ext_vector_type(2)));
typedef __attribute__((address_space(1))) unsigned long long gu64;
typedef __amdgpu_buffer_rsrc_t srd_t;
#define LAS __attribute__((address_space(3)))
#define DI __device__ __forceinline__
#define GAP() __builtin_amdgcn_sched_barrier(0)

constexpr int SQ = 144, DM = 640, NH = 32, SK = 1024, DH = 128;
constexpr int HD = NH * DH;
constexpr float QSCALE = 0.12751743082f;
constexpr unsigned SPIN_MAX = 1u << 18;
constexpr unsigned CODE_LINES = 240;
constexpr unsigned OOR = 0x40000000u;
#define QP 58
#define QE 4
#define DT 115
#define DT1 DT
#define NI 0u
#define DTF 105
#define FINV 1
#define V2E 1
#define DTE 15
#define KA0 1
#define KA1 7
#define EARLY 1
#define FORCE 0
#define STAMPS 0

constexpr unsigned SLOT = 16384;
constexpr unsigned KR = 0, VR = 49152;
constexpr unsigned WS0 = 32768;
constexpr unsigned XS0 = 98304, XSZ = 18432;
constexpr unsigned QA = 135168, QB = 98304;
constexpr unsigned PB = 98304;
constexpr unsigned STATS = 159744;
constexpr unsigned SPARE = 155648;
constexpr unsigned TAGW = 163832;
constexpr unsigned BSZ = 40960;
constexpr unsigned OWN = 3 * BSZ;
constexpr unsigned KEEP = BSZ, SEEN = BSZ + 8192;
constexpr unsigned LDS_BYTES = 163840;
constexpr unsigned OSZ = 36864;
constexpr size_t WS_MBOX = 256 * OSZ;
constexpr unsigned MBOX = 49152;
constexpr size_t WS_FLAG = WS_MBOX + 256 * (size_t)MBOX;

template <class F, int... Is> DI void static_for_(F&& f, std::integer_sequence<int, Is...>) { (f(std::integral_constant<int, Is>{}), ...); }
template <int N, class F> DI void static_for(F&& f) { static_for_(f, std::make_integer_sequence<int, N>{}); }
DI unsigned rfl(unsigned x) { return __builtin_amdgcn_readfirstlane(x); }
DI unsigned cvtpk(float lo, float hi) { f32x2 v = {lo, hi}; bf16x2 b = __builtin_convertvector(v, bf16x2); return __builtin_bit_cast(unsigned, b); }
DI u32x4 pack8(const f32x4& x, const f32x4& y) { return u32x4{cvtpk(x[0], x[1]), cvtpk(x[2], x[3]), cvtpk(y[0], y[1]), cvtpk(y[2], y[3])}; }
DI bf16x8 lds128(unsigned a) { return *(LAS bf16x8*)a; }
DI bf16x4 ldstr(unsigned a) { return __builtin_amdgcn_ds_read_tr16_b64_v4bf16((LAS bf16x4*)a); }
DI bf16x8 cat(bf16x4 a, bf16x4 b) { return __builtin_shufflevector(a, b, 0, 1, 2, 3, 4, 5, 6, 7); }
DI srd_t mksrd(const void* p, unsigned bytes) { return __builtin_amdgcn_make_buffer_rsrc((void*)p, (short)0, bytes, 0x00020000); }
template <int N> DI void waitvm() { asm volatile("s_waitcnt vmcnt(%0)" ::"i"(N) : "memory"); }
DI void bar() { asm volatile("s_waitcnt lgkmcnt(0)\n\ts_barrier" ::: "memory"); GAP(); }
DI void set_m0(unsigned m0) { asm volatile("s_mov_b32 m0, %0\n\ts_nop 0" ::"s"(m0) : "memory"); }
template <int OFF, bool NT> DI void dma(unsigned voff, srd_t srd, unsigned soff) {
  if constexpr (NT) asm volatile("buffer_load_dwordx4 %0, %1, %2 offen offset:%3 nt lds" ::"v"(voff), "s"(srd), "s"(soff), "i"(OFF) : "memory");
  else asm volatile("buffer_load_dwordx4 %0, %1, %2 offen offset:%3 lds" ::"v"(voff), "s"(srd), "s"(soff), "i"(OFF) : "memory");
  GAP();
}
template <bool NT> DI void dma4(unsigned m0, const unsigned (&voff)[4], srd_t srd, unsigned soff) {
  set_m0(m0); dma<0, NT>(voff[0], srd, soff); dma<1024, NT>(voff[1], srd, soff); dma<2048, NT>(voff[2], srd, soff); dma<3072, NT>(voff[3], srd, soff);
}
DI void dma_half(unsigned m0, unsigned voff, srd_t srd, unsigned soff) {
  asm volatile("s_mov_b32 m0, %0\n\ts_mov_b32 exec_hi, 0\n\tbuffer_load_dwordx4 %1, %2, %3 offen lds\n\ts_mov_b32 exec_hi, -1" ::"s"(m0), "v"(voff), "s"(srd), "s"(soff) : "memory");
}
DI void touch(unsigned& d, srd_t s, unsigned voff) { asm volatile("buffer_load_dword %0, %1, %2, 0 offen" : "=&v"(d) : "v"(voff), "s"(s) : "memory"); GAP(); }
DI void keep(unsigned d) { asm volatile("" ::"v"(d)); }
DI void store_wt(u32x4 v, srd_t srd, unsigned voff, unsigned soff) { __builtin_amdgcn_raw_buffer_store_b128(v, srd, voff, soff, 16); GAP(); asm volatile("s_nop 1"); GAP(); }
DI bf16x8 load_wt(srd_t srd, unsigned voff, unsigned soff) { return __builtin_bit_cast(bf16x8, __builtin_amdgcn_raw_buffer_load_b128(srd, voff, soff, 16)); }
DI unsigned long long flag_get(gu64* f) { return __hip_atomic_load(f, __ATOMIC_RELAXED, __HIP_MEMORY_SCOPE_AGENT); }
DI void flag_set(gu64* f, unsigned long long v) { __hip_atomic_store(f, v, __ATOMIC_RELAXED, __HIP_MEMORY_SCOPE_AGENT); }
struct Poll {
  unsigned long long v0, v1, v2;
  static DI void read(unsigned long long& v, gu64* f, unsigned off) {
    asm volatile("s_cmp_lg_u32 %2, 0\n\ts_cbranch_scc1 1f\n\tglobal_load_dwordx2 %0, %1, off sc1\n1:" : "=&v"(v) : "v"(f), "s"(off) : "scc", "memory"); GAP();
  }
  DI void spin(gu64* f, unsigned long long tag, unsigned off) {
    unsigned n = SPIN_MAX;
#define SPIN(V) "s_waitcnt vmcnt(2)\n\tv_cmp_ne_u64 vcc, %5, " V "\n\ts_cbranch_vccz 2f\n\tglobal_load_dwordx2 " V ", %4, off sc1\n\t"
    asm volatile("s_cmp_lg_u32 %6, 0\n\ts_cbranch_scc1 2f\n1:\n\t" SPIN("%0") SPIN("%1") SPIN("%2") "s_sub_u32 %3, %3, 1\n\ts_cbranch_scc0 1b\n2:"
                 : "+v"(v0), "+v"(v1), "+v"(v2), "+s"(n) : "v"(f), "s"(tag), "s"(off) : "vcc", "scc", "memory");
#undef SPIN
  }
  DI void done() { asm volatile("" ::"v"(v0), "v"(v1), "v"(v2)); }
  DI void watch(gu64* f, unsigned long long tag, unsigned lds) {
    unsigned n = SPIN_MAX, t, m;
#define WATCH(V) "s_waitcnt vmcnt(2)\n\tv_cmp_eq_u64 vcc, %7, " V "\n\ts_and_b32 %5, vcc_lo, 0x7f\n\tv_mov_b32 %4, %5\n\tds_write_b32 %8, %4\n\ts_cmp_eq_u32 %5, 0x7f\n\ts_cbranch_scc1 2f\n\tglobal_load_dwordx2 " V ", %6, off sc1\n\t"
    asm volatile("global_load_dwordx2 %0, %6, off sc1\n\ts_sleep 6\n\tglobal_load_dwordx2 %1, %6, off sc1\n\ts_sleep 6\n\tglobal_load_dwordx2 %2, %6, off sc1\n"
                 "1:\n\t" WATCH("%0") WATCH("%1") WATCH("%2") "s_sub_u32 %3, %3, 1\n\ts_cbranch_scc0 1b\n"
                 "2:\n\ts_waitcnt vmcnt(0) lgkmcnt(0)"
                 : "=&v"(v0), "=&v"(v1), "=&v"(v2), "+s"(n), "=&v"(t), "=&s"(m) : "v"(f), "s"(tag), "v"(lds) : "vcc", "scc", "memory");
#undef WATCH
  }
};
DI float swap32_add(float x) { auto r = __builtin_amdgcn_permlane32_swap(__float_as_uint(x), __float_as_uint(x), false, false); return __uint_as_float(r[0]) + __uint_as_float(r[1]); }
DI float swap16_add(float x) { auto r = __builtin_amdgcn_permlane16_swap(__float_as_uint(x), __float_as_uint(x), false, false); return __uint_as_float(r[0]) + __uint_as_float(r[1]); }
DI float swap32_max(float x) { auto r = __builtin_amdgcn_permlane32_swap(__float_as_uint(x), __float_as_uint(x), false, false); return fmaxf(__uint_as_float(r[0]), __uint_as_float(r[1])); }
DI float swap16_max(float x) { auto r = __builtin_amdgcn_permlane16_swap(__float_as_uint(x), __float_as_uint(x), false, false); return fmaxf(__uint_as_float(r[0]), __uint_as_float(r[1])); }
DI float fadd(float a, float b) { float r; asm("v_add_f32_e32 %0, %1, %2" : "=v"(r) : "v"(a), "v"(b)); return r; }
DI float fmul(float a, float b) { float r; asm("v_mul_f32_e32 %0, %1, %2" : "=v"(r) : "v"(a), "v"(b)); return r; }
DI f32x4 scale(const f32x4& v, float s) { return f32x4{fmul(v[0], s), fmul(v[1], s), fmul(v[2], s), fmul(v[3], s)}; }
DI unsigned fresh_lane() { unsigned l; asm volatile("v_mbcnt_lo_u32_b32 %0, -1, 0\n\tv_mbcnt_hi_u32_b32 %0, -1, %0" : "=v"(l)); return l; }
DI void mma(f32x4& d, bf16x8 a, bf16x8 b) { asm volatile("v_mfma_f32_16x16x32_bf16 %0, %1, %2, %0" : "+v"(d) : "v"(a), "v"(b)); GAP(); }
DI void mma0(f32x4& d, bf16x8 a, bf16x8 b) { asm volatile("v_mfma_f32_16x16x32_bf16 %0, %1, %2, 0" : "=&v"(d) : "v"(a), "v"(b)); GAP(); }
template <bool FIRST> DI void mmaf(f32x4& d, bf16x8 a, bf16x8 b) { if constexpr (FIRST) mma0(d, a, b); else mma(d, a, b); }
const f32x4 kZero4 = {0.f, 0.f, 0.f, 0.f};
DI void zero(f32x4& d) { d = kZero4; asm volatile("" : "+v"(d)); }
DI unsigned clock_now() { unsigned long long t; asm volatile("s_memrealtime %0\n\ts_waitcnt lgkmcnt(0)" : "=s"(t)::"memory"); return (unsigned)t; }
DI void until(unsigned t) { asm volatile("1:\n\ts_memrealtime vcc\n\ts_waitcnt lgkmcnt(0)\n\ts_sub_u32 vcc_lo, vcc_lo, %0\n\ts_cmp_lt_i32 vcc_lo, 0\n\ts_cbranch_scc1 1b" ::"s"(t) : "vcc", "scc", "memory"); }

#define STAMP(i) do {} while (0)
#define STAMP_END do {} while (0)
#define TSTAMP(i) STAMP(i)

struct Ctx {
  unsigned ldsb, w, w4, b, h, wi;
  unsigned long long tag;
  char* ws; u16* out; const u16 *wo, *kc, *vc;
  srd_t srdK, srdV, srdB;
  unsigned koff[4], voff[4], boff[5];
  unsigned ba[4][2], ao[4];
};
DI void dma_k(const Ctx& c, unsigned t) { dma4<true>(c.ldsb + KR + (t % 3u) * SLOT + c.w4 * 4096u, c.koff, c.srdK, t * SLOT); }
DI void dma_v(const Ctx& c, unsigned t) { dma4<true>(c.ldsb + VR + ((t + 2u) % 3u) * SLOT + c.w4 * 4096u, c.voff, c.srdV, t * SLOT); }
DI srd_t srd_wo(const Ctx& c) { return mksrd((const char*)c.wo + (size_t)(c.h >> 2) * 512u * DM * 2u + (c.h & 3u) * 320u, 511 * DM * 2 + 320); }
template <int S, int I> DI void dma_b(const Ctx& c, unsigned rb) {
  if constexpr ((I & 3) == 0) set_m0(c.ldsb + S * BSZ + rb * 5120u + I * 1024u);
  dma<(I & 3) * 1024, false>(c.boff[I], c.srdB, ((((c.h + S) & 3u) << 7) + rb * 16u) * (DM * 2u));
}

DI void proj_init(Ctx& c, unsigned lane, unsigned nh) {
  const unsigned g4 = lane >> 4, q4 = (lane & 15u) >> 2, p = lane & 3u;
#pragma unroll
  for (int ks = 0; ks < 4; ++ks) {
    const unsigned ka = (ks + c.w4) & 3u;
    c.ao[ks] = lane * 16u + ka * 1024u;
#pragma unroll
    for (int e = 0; e < 2; ++e) c.ba[ks][e] = c.ldsb + (32u * ka + 4u * g4 + q4) * 320u + 8u * p + (((5u * nh + e) ^ (g4 & 1u)) - e) * 32u;
    asm volatile("" : "+v"(c.ao[ks]), "+v"(c.ba[ks][0]), "+v"(c.ba[ks][1]));
  }
#pragma unroll
  for (int i = 0; i < 5; ++i) {
    const unsigned ci = 64u * i + lane, row = ci / 20u;
    c.boff[i] = row * (DM * 2u) + (((ci - 20u * row) ^ (((row >> 2) & 1u) << 1)) << 4) - (i & 3) * 1024u;
    asm volatile("" : "+v"(c.boff[i]));
  }
}

template <int NQ> struct Att {
  f32x4 O[NQ][2], S[NQ];
  bf16x8 Q[NQ][4], K[4], V[2][2], P[NQ][2];
  u32x2 pk;
  float l[NQ];
  unsigned ka[4], va[2], pw, pr;
  unsigned rs, m0v;
  unsigned warm, wv, due;
};
template <int NQ> constexpr bool blk_pv(int b) { return b >= 2 * NQ - 3 ? true : (b >= 3 && (b & 1)); }
template <int NQ> constexpr int blk_tile(int b) { return b >= 2 * NQ - 3 ? b - NQ : b < 3 ? b : (b & 1) ? (b - 3) / 2 : (b + 2) / 2; }
template <int NQ> constexpr int pv_start(int t) { for (int b = 0; b < 2 * NQ; ++b) if (blk_pv<NQ>(b) && blk_tile<NQ>(b) == t) return 4 * b; return -1; }
template <int NQ, int T, int F, unsigned BUF> DI void sm_op(Att<NQ>& a) {
  if constexpr (F == 0 || F == 1) a.S[T][F] = __builtin_amdgcn_exp2f(a.S[T][F]);
  if constexpr (F == 3 || F == 4) a.S[T][F - 1] = __builtin_amdgcn_exp2f(a.S[T][F - 1]);
  if constexpr (F == 2) { a.l[T] = fadd(a.l[T], a.S[T][0]); GAP(); a.pk[0] = cvtpk(a.S[T][0], a.S[T][1]); }
  if constexpr (F == 5) { a.l[T] = fadd(a.l[T], a.S[T][1]); GAP(); a.pk[1] = cvtpk(a.S[T][2], a.S[T][3]); }
  if constexpr (F == 6) { *(LAS u32x2*)(a.pw + BUF + T * 2048) = a.pk; GAP(); a.l[T] = fadd(a.l[T], a.S[T][2]); GAP(); a.l[T] = fadd(a.l[T], a.S[T][3]); }
  GAP();
}
template <int NQ, unsigned BUF, int LO, int N> DI void sm_run(Att<NQ>& a) { static_for<N>([&](auto n) __attribute__((always_inline)) { sm_op<NQ, (LO + n) / 7, (LO + n) % 7, BUF>(a); }); }
template <int NQ, int PAR, int MODE, int N> DI void gap(Ctx& c, Att<NQ>& a, unsigned i) {
  constexpr int G = 8 * NQ, B = N >> 2, X = N & 3, T = blk_tile<NQ>(B);
  constexpr unsigned CUR = PAR ? 0u : XSZ, NXT = PAR ? XSZ : 0u;
  if constexpr (blk_pv<NQ>(B)) mma(a.O[T][X >> 1], a.V[X & 1][X >> 1], a.P[T][X & 1]); else mmaf<X == 0>(a.S[T], a.K[X], a.Q[T][X]);
  if constexpr (N >= 3 && (N - 3) % (G / 4) == 0) __builtin_amdgcn_s_setprio(3 - (N - 3) / (G / 4));
  if constexpr (N + G - 6 < 7 * NQ) sm_op<NQ, NQ - 1, N + G - 6 - 7 * (NQ - 1), CUR>(a);
  if constexpr (N == 2) {
    asm volatile("" : "+s"(a.rs));
    if constexpr (NQ == 4) a.m0v = c.ldsb + VR + c.w4 * 4096u + a.rs;
    const unsigned d = a.rs == 2u * SLOT ? -2 * (int)SLOT : SLOT;
    a.rs += d;
#pragma unroll
    for (int s = 0; s < 4; ++s) a.ka[s] += d;
    a.va[0] += d; a.va[1] += d;
    GAP();
  }
  if constexpr (N == 3) {
    if constexpr (NQ == 4) {
      waitvm<MODE <= 12 ? 8 : FINV == 0 ? 5 : MODE == 13 ? 4 : 1>();
      if constexpr (MODE <= DTE) { until(a.due); a.due += MODE >= 12 ? DTF : i < NI ? DT1 : DT; }
    }
    bar();
  }
  if constexpr (N >= 3 && N <= 6) { constexpr int f = N - 3; const unsigned ad = a.va[f >> 1] + (f & 1) * 8192; a.V[f & 1][f >> 1] = cat(ldstr(ad), ldstr(ad + 4096)); GAP(); }
  if constexpr (N >= 6) sm_op<NQ, (N - 6) / 7, (N - 6) % 7, NXT>(a);
#pragma unroll
  for (int t = 0; t < NQ; ++t)
#pragma unroll
    for (int ks = 0; ks < 2; ++ks)
      if (N == pv_start<NQ>(t) - (t == 0 ? 5 : 6) + ks) { a.P[t][ks] = lds128(a.pr + CUR + t * 2048 + ks * 1024); GAP(); }
  if constexpr (B == 2 * NQ - 4 && MODE != 14) { a.K[X] = lds128(a.ka[X]); GAP(); }
  if constexpr (NQ == 4) {
    if constexpr (MODE <= 13 && N >= 4 && N <= 10 && !(N & 1)) {
      constexpr int j = (N - 4) / 2;
      constexpr bool Z = V2E && MODE == 0 && PAR == 0;
      if (j == 0) set_m0(Z && i == 0u ? c.ldsb + SPARE : a.m0v);
      dma<j * 1024, true>(c.voff[j], c.srdV, Z && i == 0u ? OOR : (i + 2u) * SLOT);
    }
    if constexpr (MODE == 0 && N >= 16 && !(N & 3)) { constexpr int j = (N - 16) / 4; if (j == 0) set_m0(a.m0v - (VR - KR)); dma<j * 1024, true>(c.koff[j], c.srdK, (i + 4u) * SLOT); }
    if constexpr (MODE == (FINV ? 13 : 12) && N == 17) touch(a.warm, mksrd((const char*)c.wo + ((size_t)(c.h >> 2) * 512u + c.wi * 16u) * (DM * 2u), 16 * DM * 2), a.wv);
    if constexpr (FINV == 0) {
      if constexpr (MODE == 13 && N >= 22 && !(N & 1)) dma_b<0, (N - 22) / 2>(c, 4u + c.w4);
      if constexpr (MODE == 14 && N >= 4 && (N - 4) % 3 == 0) dma_b<(N - 4) / 15, (N - 4) / 3 % 5>(c, c.w4);
    } else if constexpr (MODE == 14 && N >= 22) dma_b<0, (N - 22) % 5>(c, 2u * c.w4 + (N - 22) / 5);
  }
}
template <int NQ, int PAR, int MODE> DI void step(Ctx& c, Att<NQ>& a, unsigned i) { static_for<8 * NQ>([&](auto n) __attribute__((always_inline)) { gap<NQ, PAR, MODE, n>(c, a, i); }); }

constexpr float SUM_LO = FORCE ? 1e30f : 0x1p-24f, SUM_HI = 0x1p40f;
template <int NQ> DI void attend_with_max(const Ctx& c, Att<NQ>& a, float (&inv)[NQ], unsigned lane) {
  const unsigned r16 = lane & 15u, g4 = lane >> 4;
  const u16* kp = c.kc + (size_t)(c.b * NH + c.h) * SK * DH + r16 * DH + g4 * 8u;
  const u16* vp = c.vc + (size_t)(c.b * NH + c.h) * SK * DH + g4 * 4u * DH + c.w4 * 32u + r16;
  float m[NQ], l[NQ];
#pragma unroll
  for (int t = 0; t < NQ; ++t) { m[t] = -__builtin_inff(); l[t] = 0.f; a.O[t][0] = kZero4; a.O[t][1] = kZero4; }
#pragma nounroll
  for (int kb = 0; kb < SK / 32; ++kb, kp += 32 * DH, vp += 32 * DH) {
    bf16x8 kf[2][4]; u32x4 vf[2];
#pragma unroll
    for (int i = 0; i < 8; ++i) kf[i >> 2][i & 3] = *(const bf16x8*)(kp + (i >> 2) * 16 * DH + (i & 3) * 32);
#pragma unroll
    for (int e = 0; e < 2; ++e)
#pragma unroll
      for (int i = 0; i < 4; ++i) { const u16* q = vp + ((i >> 1) * 16 + (i & 1) * 2) * DH + e * 16; vf[e][i] = q[0] | ((unsigned)q[DH] << 16); }
#pragma unroll
    for (int t = 0; t < NQ; ++t) {
      f32x4 sc[2] = {kZero4, kZero4};
#pragma unroll
      for (int i = 0; i < 8; ++i) sc[i >> 2] = __builtin_amdgcn_mfma_f32_16x16x32_bf16(kf[i >> 2][i & 3], a.Q[t][i & 3], sc[i >> 2], 0, 0, 0);
      float mx = m[t];
#pragma unroll
      for (int i = 0; i < 8; ++i) mx = fmaxf(mx, sc[i >> 2][i & 3]);
      mx = swap32_max(swap16_max(mx));
      const float f = __builtin_amdgcn_exp2f(m[t] - mx);
      m[t] = mx;
      float sum = 0.f;
#pragma unroll
      for (int i = 0; i < 8; ++i) { sc[i >> 2][i & 3] = __builtin_amdgcn_exp2f(sc[i >> 2][i & 3] - mx); sum += sc[i >> 2][i & 3]; }
      l[t] = l[t] * f + sum;
      const u32x4 pf = pack8(sc[0], sc[1]);
#pragma unroll
      for (int e = 0; e < 2; ++e) a.O[t][e] = __builtin_amdgcn_mfma_f32_16x16x32_bf16(__builtin_bit_cast(bf16x8, vf[e]), __builtin_bit_cast(bf16x8, pf), a.O[t][e] * f, 0, 0, 0);
      GAP();
    }
  }
#pragma unroll
  for (int t = 0; t < NQ; ++t) inv[t] = __builtin_amdgcn_rcpf(swap32_add(swap16_add(l[t])));
}

template <int NQ> DI void att_init(Ctx& c, Att<NQ>& a, unsigned lane) {
  proj_init(c, lane, NQ == 4);
  if constexpr (NQ == 4) { a.wv = lane < 40u ? (c.w4 * 40u + lane) * 128u : OOR; asm volatile("" : "+v"(a.wv)); }
  const unsigned r16 = lane & 15u, g4 = lane >> 4, q4 = r16 >> 2, p = lane & 3u;
#pragma unroll
  for (int s = 0; s < 4; ++s) { a.ka[s] = c.ldsb + KR + (16u * c.w4 + r16) * 256u + (((4u * s + g4) ^ r16) << 4); asm volatile("" : "+v"(a.ka[s])); }
#pragma unroll
  for (int e = 0; e < 2; ++e) { a.va[e] = c.ldsb + VR + SLOT + (4u * g4 + q4) * 256u + (((4u * c.w4 + 2u * e + (p >> 1)) ^ ((q4 << 2) | ((g4 & 1u) << 1))) << 4) + 8u * (p & 1u); asm volatile("" : "+v"(a.va[e])); }
  a.pr = c.ldsb + PB + (NQ == 4 ? 5u : 0u) * 2048u + lane * 16u;
  a.pw = a.pr + (c.w4 >> 1) * 1024u + (c.w4 & 1u) * 8u;
  asm volatile("" : "+v"(a.pr), "+v"(a.pw));
  a.rs = SLOT;
#pragma unroll
  for (int t = 0; t < NQ; ++t) { zero(a.O[t][0]); zero(a.O[t][1]); a.l[t] = 0.f; asm volatile("" : "+v"(a.l[t])); }
}

template <int NQ> DI void attend(Ctx& c, Att<NQ>& a, unsigned lane) {
  constexpr bool L = NQ == 4;
  constexpr unsigned T0 = L ? 5u : 0u;
  const unsigned ldsb = c.ldsb, w4 = c.w4;
  {
    unsigned qa = ldsb + (L ? QB : QA) + lane * 16u; asm volatile("" : "+v"(qa));
#pragma unroll
    for (int t = 0; t < NQ; ++t)
#pragma unroll
      for (int s = 0; s < 4; ++s) { a.Q[t][s] = lds128(qa + t * 4096 + s * 1024); GAP(); }
  }
  const u32x2 tg = *(LAS u32x2*)(ldsb + TAGW);
  GAP();
  static_for<4 * NQ>([&](auto n) __attribute__((always_inline)) {
    constexpr int T = n >> 2, X = n & 3;
    mmaf<X == 0>(a.S[T], a.K[X], a.Q[T][X]);
    if constexpr (n == 8) asm volatile("s_nop 2");
    if constexpr (T >= 2) { sm_op<NQ, T - 2, 2 * X, XSZ>(a); if constexpr (X < 3) sm_op<NQ, T - 2, 2 * X + 1, XSZ>(a); }
    if constexpr (T == NQ - 1) { a.ka[X] += SLOT; a.K[X] = lds128(a.ka[X]); GAP(); }
  });
  sm_run<NQ, XSZ, 7 * (NQ - 2), NQ + 8>(a);
  c.tag = ((unsigned long long)rfl(tg[1]) << 32) | rfl(tg[0]);
  if constexpr (L) a.due = clock_now();
  STAMP(3);
#pragma nounroll
  for (unsigned i = 0; i < (L ? 12u : 14u); i += 2u) {
    step<NQ, 0, 0>(c, a, i);
    step<NQ, 1, 0>(c, a, i + 1u);
  }
  if constexpr (L) {
    c.srdB = srd_wo(c);
    step<NQ, 0, 12>(c, a, 12u);
    step<NQ, 1, 13>(c, a, 13u);
  }
  step<NQ, 0, 14>(c, a, 14u);
#pragma unroll
  for (int f = 0; f < 4; ++f) { const unsigned ad = a.va[f >> 1] + SLOT + (f & 1) * 8192; a.V[f & 1][f >> 1] = cat(ldstr(ad), ldstr(ad + 4096)); GAP(); }
  sm_run<NQ, 0u, 8 * NQ - 6, 6 - NQ>(a);
  lane = fresh_lane();
  const unsigned sa = ldsb + STATS + T0 * 256u + (lane & 15u) * 16u;
#pragma unroll
  for (int t = 0; t < NQ; ++t) {
    const float l = swap32_add(swap16_add(a.l[t]));
    *(LAS float*)(sa + t * 256 + w4 * 4u) = l;
  }
  if constexpr (L) { if (DTE >= 15) until(a.due); } else c.srdB = srd_wo(c);
  bar();
  TSTAMP(5);
  f32x4 ls[NQ];
#pragma unroll
  for (int t = 0; t < NQ; ++t) {
    a.P[t][0] = lds128(a.pr + t * 2048); a.P[t][1] = lds128(a.pr + t * 2048 + 1024); GAP();
    if (t > 0) { ls[t - 1] = *(LAS f32x4*)(sa + (t - 1) * 256); GAP(); }
  }
  ls[NQ - 1] = *(LAS f32x4*)(sa + (NQ - 1) * 256); GAP();
  const srd_t srdO = mksrd(c.ws + (size_t)(c.b * NH + c.h) * OSZ, OSZ);
  float inv[NQ];
  auto put = [&](int t) {
    const u32x4 f = pack8(scale(a.O[t][0], inv[t]), scale(a.O[t][1], inv[t]));
    store_wt(f, srdO, lane * 16u, (T0 + t) * 4096u + w4 * 1024u);
    *(LAS u32x4*)(ldsb + OWN + (T0 + t) * 4096u + w4 * 1024u + lane * 16u) = f; GAP();
  };
  bool ok = true;
  static_for<4 * NQ>([&](auto n) __attribute__((always_inline)) {
    constexpr int t = n >> 2, x = n & 3;
    mma(a.O[t][x >> 1], a.V[x & 1][x >> 1], a.P[t][x & 1]);
    if constexpr ((L || FINV) && !(n & 1) && n < 10) dma_b<1, n / 2>(c, FINV ? c.w : 4u + w4);
    if constexpr (x == 3) {
      const float sum = fadd(fadd(ls[t][0], ls[t][1]), fadd(ls[t][2], ls[t][3]));
      ok = ok && sum >= SUM_LO && sum <= SUM_HI;
      float r = __builtin_amdgcn_rcpf(sum); asm volatile("" : "+v"(r)); inv[t] = r; GAP();
      if (t >= 2) put(t - 2);
    }
  });
  STAMP(6);
  put(NQ - 2);
  put(NQ - 1);
  if (__builtin_expect(!__all(ok), 0)) {
    waitvm<0>();
    attend_with_max<NQ>(c, a, inv, lane);
#pragma unroll
    for (int t = 0; t < NQ; ++t) put(t);
  }
  if constexpr (L) { waitvm<NQ + (FINV ? 5 : 10)>(); keep(a.warm); }
}

DI void project(Ctx& c, const unsigned nh) {
  const unsigned lane = fresh_lane();
  asm volatile("" : "+s"(c.w), "+s"(c.w4), "+s"(c.h), "+s"(c.b));
  c.srdB = srd_wo(c);
  const unsigned ldsb = c.ldsb, w = c.w, w4 = c.w4, h = c.h, b = c.b;
  const unsigned r16 = lane & 15u, g4 = lane >> 4, q4 = r16 >> 2, p = lane & 3u;
  const unsigned g = h >> 2, j = h & 3u;
  gu64* const flags = (gu64*)(c.ws + WS_FLAG);
  gu64* const f1 = flags + (8u + b * 32u + g * 4u + ((j + 1u + p % 3u) & 3u)) * 16u;
  gu64* const f2 = flags + (264u + b * 32u + ((g + 1u + (lane & 7u) % 7u) & 7u) * 4u + j) * 16u;
  const srd_t srdO = mksrd(c.ws + (size_t)(b * NH + g * 4u) * OSZ, 4 * OSZ);
  const srd_t srdM = mksrd(c.ws + WS_MBOX + (size_t)b * 32u * MBOX, 32 * MBOX);
  auto& ba = c.ba; auto& ao = c.ao;
  bf16x8 Bf[10], Af[16][2], Ar[4];
  f32x4 acc[3][5], rr[2][4];
  auto load_a = [&](int f) {
    const int s = 1 + f / 9, r = f % 9;
    const unsigned so = ((j + s) & 3u) * OSZ;
    if (r == 2) Ar[s] = load_wt(srdO, ao[0], so + 8u * 4096u);
    else { const int i = r < 2 ? r : r - 1; Af[4 * s + (i >> 1)][i & 1] = load_wt(srdO, ao[i >> 1], so + (2u * w4 + (i & 1)) * 4096u); }
    GAP();
  };
  auto box = [&](unsigned dst) { return dst == g ? OOR : (dst * 4u + j) * MBOX + ((g - dst - 1u) & 7u) * 1024u; };
  const unsigned mine = 2u * w4 + nh, mb = box(mine);
  unsigned back, got = 0;
  unsigned long long early;
  Poll poll;
  bar();
  TSTAMP(7);
#pragma unroll
  for (int ks = 0; ks < 4; ++ks) {
#pragma unroll
    for (int m = 0; m < 2; ++m) Af[ks][m] = lds128(ldsb + OWN + (2u * w4 + m) * 4096u + ao[ks]);
    if (ks == 0) {
      Ar[0] = lds128(ldsb + OWN + 8u * 4096u + ao[0]);
#pragma unroll
      for (int k = 0; k < 10; ++k) { const unsigned ad = ba[k / 5][k % 5 & 1] + k % 5 * 32; Bf[k] = cat(ldstr(ad), ldstr(ad + 5120)); }
    }
    GAP();
  }
  static_for<80>([&](auto k_) __attribute__((always_inline)) {
    constexpr int k = k_, r = k + 9;
    constexpr int u = k < 65 ? k / 5 : 13 + (k - 65) % 3, nt = k < 65 ? k % 5 : (k - 65) / 3;
    constexpr int ru = r < 65 ? r / 5 : 13 + (r - 65) % 3, rn = r < 65 ? r % 5 : (r - 65) / 3;
    constexpr bool refill = k >= 1 && r < 80;
    if constexpr (k == 10) {
      waitvm<5>();
      bar();
      if (w == 7u && lane == 0) flag_set(flags + (8u + b * 32u + h) * 16u, c.tag);
      TSTAMP(8);
      asm volatile("buffer_load_dword %0, %1, %2, %3 offen sc1" : "=&v"(back) : "v"(lane < 36u ? (w * 36u + lane) * 128u : OOR), "s"(srdO), "s"(j * OSZ) : "memory");
    }
    if constexpr (k == 15) Poll::read(poll.v0, f1, w);
    if constexpr (k == 18) Poll::read(poll.v1, f1, w);
    if constexpr (k == 20) {
      Poll::read(poll.v2, f1, w);
      poll.spin(f1, c.tag, w);
      if (w != 0u) waitvm<0>();
      bar();
      TSTAMP(9);
      load_a(0); load_a(1); load_a(2); load_a(3); load_a(4);
      TSTAMP(10);
    }
    if constexpr (k % 5 == 0) __builtin_amdgcn_s_setprio(k < 20 ? 1 - (k / 5 & 1) : 3 - (k - 20) * 4 / 55 % 4);
    if constexpr (refill && ru >= 4 && rn == 0) { ba[ru & 3][0] += BSZ; ba[ru & 3][1] += BSZ; asm volatile("" : "+v"(ba[ru & 3][0]), "+v"(ba[ru & 3][1])); GAP(); }
    const unsigned ad = ba[ru & 3][rn & 1] + rn * 32;
    bf16x4 lo, hi;
    mmaf<u == 0>(acc[0][nt], Bf[k % 10], Af[u][0]);
    if constexpr (k == 21) { poll.done(); keep(back); }
    if constexpr (k == 75) bar();
    if constexpr (refill) { lo = ldstr(ad); GAP(); }
    mmaf<u == 0>(acc[1][nt], Bf[k % 10], Af[u][1]);
    if constexpr (refill) { hi = ldstr(ad + 5120); GAP(); }
    if constexpr ((u & 3) == 0) mmaf<u == 0>(acc[2][nt], Bf[k % 10], Ar[u >> 2]);
    if constexpr (refill) Bf[r % 10] = cat(lo, hi);
    if constexpr (k < 5) dma_b<2, k>(c, w);
    if constexpr (k >= 10 && k < 15) dma_b<3, k - 10>(c, w);
    if constexpr (k >= 20 && !(k % 5 & 1) && 5 + 3 * (k / 5 - 4) + k % 5 / 2 < 27) load_a(5 + 3 * (k / 5 - 4) + k % 5 / 2);
    if constexpr (EARLY && k == 53) { early = flag_get(f2); GAP(); }
    if constexpr (EARLY && k == 69) { got = (unsigned)__builtin_amdgcn_ballot_w64(early == c.tag) & 0x7fu; asm volatile("" : "+s"(got)::"memory"); GAP(); }
    if constexpr (k >= 66 && k < 71) { *(LAS f32x4*)(ldsb + w4 * 10240u + (5u * nh + (k - 66)) * 1024u + lane * 16u) = acc[2][k - 66]; GAP(); }
    if constexpr (k >= 75 && k < 79) {
      const unsigned ra = ldsb + (k - 75) * 10240u + (w < 4u ? w : 4u) * 2048u + lane * 16u;
      rr[0][k - 75] = *(LAS f32x4*)ra; rr[1][k - 75] = *(LAS f32x4*)(ra + 1024u); GAP();
    }
    if constexpr (k >= 65 && u == 15 && nt >= 1) { if (k > 70) asm volatile("s_nop 3"); store_wt(pack8(acc[0][nt - 1], acc[1][nt - 1]), srdM, lane * 16u, mb + (nt - 1) * 8192u); }
  });
  TSTAMP(11);
  {
    const u32x4 r8 = pack8((rr[0][0] + rr[0][1]) + (rr[0][2] + rr[0][3]), (rr[1][0] + rr[1][1]) + (rr[1][2] + rr[1][3]));
    store_wt(r8, srdM, lane * 16u, w < 5u ? box(w) + 5u * 8192u : OOR);
    store_wt(pack8(acc[0][4], acc[1][4]), srdM, lane * 16u, mb + 4u * 8192u);
    if (w == g) *(LAS u32x4*)(ldsb + KEEP + 5u * 1024u + lane * 16u) = r8;
    if (mine == g) {
#pragma unroll
      for (int nt = 0; nt < 5; ++nt) *(LAS u32x4*)(ldsb + KEEP + nt * 1024u + lane * 16u) = pack8(acc[0][nt], acc[1][nt]);
    }
    if (w == 6u) *(LAS unsigned*)(ldsb + SEEN) = 0u;
  }
  const bool reader = w < 5u || (w == 5u && g < 5u);
  const unsigned so = h * MBOX + w * 8192u;
  u32x4 in[8];
#pragma unroll
  for (int i = 0; i < 7; ++i) {
    if (EARLY) asm volatile("buffer_load_dwordx4 %0, %1, %2, %3 offen sc1" : "=&v"(in[i + 1]) : "v"(lane * 16u), "s"(srdM), "s"(reader && (got >> i & 1u) ? so + i * 1024u : OOR) : "memory");
    else asm volatile("" : "=v"(in[i + 1]));
  }
  TSTAMP(12);
  waitvm<EARLY ? 7 : 0>();
  bar();
  if (w == 7u && lane == 0) flag_set(flags + (264u + b * 32u + h) * 16u, c.tag);
  TSTAMP(13);
  if (w == 6u) {
    poll.watch(f2, c.tag, ldsb + SEEN);
    if (h == 0u && lane == 0) flag_set(flags + b * 16u, c.tag);
  }
  if (reader) {
    in[0] = *(LAS u32x4*)(ldsb + KEEP + w * 1024u + lane * 16u);
    {
      unsigned n = SPIN_MAX * 8u, m, t;
#define FETCH(I, V, O) "s_bitcmp1_b32 %9, " #I "\n\ts_cbranch_scc0 3f\n\tbuffer_load_dwordx4 %" #I ", " V ", %14, %15 offen offset:" #O " sc1\n3:\n\t"
      asm volatile("s_cmp_eq_u32 %7, 0x7f\n\ts_cbranch_scc1 4f\n1:\n\tds_read_b32 %10, %11\n\ts_waitcnt lgkmcnt(0)\n\tv_readfirstlane_b32 %9, %10\n\ts_andn2_b32 %9, %9, %7\n\ts_cbranch_scc0 2f\n\ts_or_b32 %7, %7, %9\n\t"
                   FETCH(0, "%12", 0) FETCH(1, "%12", 1024) FETCH(2, "%12", 2048) FETCH(3, "%12", 3072) FETCH(4, "%13", 0) FETCH(5, "%13", 1024) FETCH(6, "%13", 2048)
                   "s_cmp_eq_u32 %7, 0x7f\n\ts_cbranch_scc1 4f\n"
                   "2:\n\ts_sleep 1\n\ts_sub_u32 %8, %8, 1\n\ts_cbranch_scc0 1b\n"
                   "4:\n\ts_waitcnt vmcnt(0)"
                   : "+v"(in[1]), "+v"(in[2]), "+v"(in[3]), "+v"(in[4]), "+v"(in[5]), "+v"(in[6]), "+v"(in[7]), "+s"(got), "+s"(n), "=&s"(m), "=&v"(t)
                   : "v"(ldsb + SEEN), "v"(lane * 16u), "v"(lane * 16u + 4096u), "s"(srdM), "s"(so) : "scc", "memory");
#undef FETCH
    }
    TSTAMP(14);
    float v[8];
#pragma unroll
    for (int e = 0; e < 8; ++e) {
      float t[8];
#pragma unroll
      for (int i = 0; i < 8; ++i) t[i] = __uint_as_float((e & 1) ? in[i][e >> 1] & 0xffff0000u : in[i][e >> 1] << 16);
      v[e] = ((t[0] + t[1]) + (t[2] + t[3])) + ((t[4] + t[5]) + (t[6] + t[7]));
    }
    const unsigned row0 = w < 5u ? 32u * (g >> 1) : 128u, col0 = w < 5u ? 80u * (g & 1u) + 16u * w : 32u * g, second = w < 5u ? 16u * DM : 16u;
    u16* const o = c.out + (size_t)(b * SQ + row0 + r16) * DM + 160u * j + col0 + 4u * g4;
    *(u32x2*)o = u32x2{cvtpk(v[0], v[1]), cvtpk(v[2], v[3])};
    *(u32x2*)(o + second) = u32x2{cvtpk(v[4], v[5]), cvtpk(v[6], v[7])};
  }
  STAMP_END;
}

constexpr int q_rest(int sg) { return 4 * ((sg < 7) + (sg == KA0) + (sg == KA1) + (sg >= 7)); }
constexpr int q_all(int sg) { return (sg == 0 ? 4 : 0) + (sg < 8 ? 5 : 0) + q_rest(sg); }
constexpr int q_wait(int sg) { return sg == 0 ? 11 : sg == 1 ? 2 + q_all(0) : q_rest(sg - 2) + q_all(sg - 1); }
static_assert(KA0 <= KA1 && KA1 <= 7, "K(0), K(1), V(0), K(2), V(1) in this order");

__global__ __launch_bounds__(512, 2) void fused_kernel(const u16* x, const u16* kc, const u16* vc,
                                                       const u16* wq, const u16* wo, char* ws, u16* out) {
  extern __shared__ __attribute__((aligned(16))) char lds[];
  unsigned long long pc; asm volatile("s_getpc_b64 %0" : "=s"(pc));
  Ctx c;
  STAMP(0);
  const unsigned ldsb = (unsigned)(size_t)(LAS char*)lds;
  const unsigned lane = threadIdx.x & 63u;
  const unsigned w = rfl(threadIdx.x >> 6);
  const unsigned bid = blockIdx.x, wi = bid >> 3;
  const unsigned h = (bid & 7u) * 4u + (wi & 3u), b = wi >> 2;
  unsigned tc;
  touch(tc, mksrd((const void*)(pc & ~255ull), CODE_LINES * 128), (wi * 8u + w) * 128u + lane * 64u + ((lane >> 3) << 24));
  const unsigned w4 = w & 3u;
  const bool loader = ((w + 2u) & 4u) != 0u;
  const unsigned r16 = lane & 15u, g4 = lane >> 4, q4 = r16 >> 2, p = lane & 3u;
  unsigned xoff[4], woff[4], xoff4 = (128u + 4u * w4 + (lane >> 3)) * (DM * 2u) + (((lane & 7u) ^ ((2u * w4 + g4) & 7u)) << 4);
#pragma unroll
  for (int i = 0; i < 4; ++i) {
    xoff[i] = (32u * w4 + 8u * i + (lane >> 3)) * (DM * 2u) + (((lane & 7u) ^ (4u * (i & 1) + g4)) << 4) - i * 1024u;
    woff[i] = (16u * w4 + 4u * i + g4) * (HD * 2u) + ((r16 ^ ((g4 << 2) | (i & 2))) << 4) - i * 1024u;
    asm volatile("" : "+v"(xoff[i]), "+v"(woff[i]));
  }
  asm volatile("" : "+v"(xoff4));
  asm volatile("" : "+s"(x), "+s"(wq), "+s"(ws));
  c.ldsb = ldsb; c.w = w; c.w4 = w4; c.b = b; c.h = h; c.wi = wi; c.ws = ws; c.out = out; c.wo = wo; c.kc = kc; c.vc = vc;
  unsigned long long epoch = 0;
  { unsigned w_ = w; asm volatile("" : "+s"(w_)); if (w_ == 0u) Poll::read(epoch, (gu64*)(ws + WS_FLAG) + b * 16u, 0u); }

  if (loader) {
    const srd_t srdX = mksrd(x + (size_t)b * SQ * DM, SQ * DM * 2);
    const srd_t srdW = mksrd(wq + h * DH, (DM * HD - h * DH) * 2);
    auto dma_w = [&](int sg) { dma4<false>(ldsb + WS0 + ((sg + 1) & 3) * SLOT + w4 * 4096u, woff, srdW, sg * (64u * HD * 2u)); };
    auto dma_x = [&](int sg) {
      dma4<false>(ldsb + XS0 + (sg % 3) * XSZ + w4 * 4096u, xoff, srdX, sg * 128u);
      dma_half(ldsb + XS0 + (sg % 3) * XSZ + 16384u + w4 * 512u, xoff4, srdX, sg * 128u);
    };
    dma_w(0); dma_x(0); dma_w(1); dma_x(1);
    unsigned tw, tx;
    const unsigned n = (h * 4u + w4) * 9u + lane;
    touch(tw, srdW, (128u + (w4 * 16u + (lane >> 1)) * 8u + b) * (HD * 2u) + (lane & 1u) * 128u + (lane < 32u ? 0u : OOR));
    touch(tx, srdX, (n >> 3) * (DM * 2u) + (2u + (n & 7u)) * 128u + (lane < 9u ? 0u : OOR));
    c.srdK = mksrd(kc + (size_t)(b * NH + h) * SK * DH, SK * DH * 2);
    c.srdV = mksrd(vc + (size_t)(b * NH + h) * SK * DH, SK * DH * 2);
#pragma unroll
    for (int i = 0; i < 4; ++i) {
      c.koff[i] = (16u * w4 + g4) * 256u + ((r16 ^ (4u * i + g4)) << 4);
      c.voff[i] = (16u * w4 + g4) * 256u + ((r16 ^ ((g4 << 2) | ((i & 1) << 1))) << 4);
    }
    unsigned tq = 0;
    static_for<10>([&](auto s_) __attribute__((always_inline)) {
      constexpr int sg = s_;
      waitvm<q_wait(sg)>();
      if (sg == 2) { keep(tc); keep(tw); keep(tx); }
      if (sg > 0 && sg <= QE) until(tq + sg * QP);
      asm volatile("s_barrier" ::: "memory");
      if (sg == 0) { tq = clock_now(); dma_w(2); }
      if (sg < 8) dma_x(sg + 2);
      if (sg < 7) dma_w(sg + 3);
      if (sg == KA0) dma_k(c, 0);
      if (sg == KA1) dma_k(c, 1);
      if (sg == 7) dma_v(c, 0);
      if (sg == 8) dma_k(c, 2);
      if (sg == 9) dma_v(c, 1);
    });
    Att<4> a;
    att_init(c, a, lane);
    waitvm<16>();
    bar();
#pragma unroll
    for (int s = 0; s < 4; ++s) a.K[s] = lds128(a.ka[s]);
    STAMP(1);
    waitvm<12>();
    bar();
    dma_k(c, 3);
    if (V2E) dma_v(c, 2);
    STAMP(2);
    attend<4>(c, a, lane);
  } else {
    const unsigned qh = w4 & 1u, dh = w4 >> 1;
    f32x4 acc[18];
#pragma unroll
    for (int m = 0; m < 18; ++m) zero(acc[m]);
    unsigned xa[2], x8[2], wa[4];
#pragma unroll
    for (int s = 0; s < 2; ++s) { xa[s] = ldsb + XS0 + (64u * qh + r16) * 128u + (((4u * s + g4) ^ (r16 >> 1)) << 4); x8[s] = xa[s] + (128u - 64u * qh) * 128u; }
#pragma unroll
    for (int dt = 0; dt < 4; ++dt) wa[dt] = ldsb + WS0 + SLOT + (8u * g4 + q4) * 256u + (((8u * dh + 2u * (dt ^ (2u * qh)) + (p >> 1)) ^ ((q4 << 2) | ((g4 & 1u) << 1))) << 4) + 8u * (p & 1u);
    bf16x8 xf[2][5];
    bf16x4 wl[2][4], wu[2][4];
    auto rd = [&](int s, int r) {
      if (r == 0) wl[s][0] = ldstr(wa[0] + s * 8192);
      else if (r == 1) wu[s][0] = ldstr(wa[0] + s * 8192 + 1024);
      else if (r < 6) xf[s][r - 2] = lds128(xa[s] + (r - 2) * 2048);
      else if (r == 6) xf[s][4] = lds128(x8[s]);
      else if (r & 1) wl[s][(r - 5) >> 1] = ldstr(wa[(r - 5) >> 1] + s * 8192);
      else wu[s][(r - 6) >> 1] = ldstr(wa[(r - 6) >> 1] + s * 8192 + 1024);
      GAP();
    };
    auto mm = [&](int s, int m) { const int dt = m < 10 ? m / 5 : (m - 2) / 4, qt = m < 10 ? m % 5 : (m - 2) % 4; mma(acc[m], cat(wl[s][dt], wu[s][dt]), xf[s][qt]); };
    Att<5> a;
    att_init(c, a, lane);
#pragma unroll
    for (int i = 0; i < 5; ++i) {
      xf[1][i] = bf16x8{}; asm volatile("" : "+v"(xf[1][i]));
      if (i < 4) { bf16x8 z = {}; asm volatile("" : "+v"(z)); wl[1][i] = __builtin_shufflevector(z, z, 0, 1, 2, 3); wu[1][i] = __builtin_shufflevector(z, z, 4, 5, 6, 7); }
    }
    asm volatile("s_nop 1");
    unsigned sl = 0, dx, dw;
#pragma nounroll
    for (unsigned sg = 0; sg < 10u; ++sg) {
#pragma unroll
      for (int m = 0; m < 18; ++m) {
        mm(1, m);
        if (m == 0) bar();
        if (m < 13) rd(0, m);
        if (m == 14) { dx = sl == 2u ? -2 * (int)XSZ : XSZ; asm volatile("" : "+s"(dx)); GAP(); }
        if (m == 15) { sl = sl == 2u ? 0u : sl + 1u; asm volatile("" : "+s"(sl)); GAP(); }
        if (m == 16) { dw = (sg & 3u) == 2u ? -3 * (int)SLOT : SLOT; asm volatile("" : "+s"(dw)); GAP(); }
      }
#pragma unroll
      for (int m = 0; m < 18; ++m) {
        mm(0, m);
        if (m < 13) rd(1, m);
        if (m == 13) { xa[0] += dx; xa[1] += dx; GAP(); }
        if (m == 14) { x8[0] += dx; x8[1] += dx; GAP(); }
        if (m == 15) { wa[0] += dw; wa[1] += dw; GAP(); }
        if (m == 16) { wa[2] += dw; wa[3] += dw; GAP(); }
      }
    }
    {
      const unsigned l2 = fresh_lane(), qo = ldsb + 2048u * dh + ((l2 >> 5) * 16u + (l2 & 15u)) * 16u + ((l2 >> 4) & 1u) * 8u;
      unsigned qw[2][3];
#pragma unroll
      for (int e = 0; e < 2; ++e) {
        qw[e][0] = qo + (e ^ qh) * 1024u + QA + qh * 16384u; qw[e][1] = qo + (e ^ qh) * 1024u + (qh ? QB - 4096u : QA); qw[e][2] = qo + (e ^ qh) * 1024u + QB + 12288u;
        asm volatile("" : "+v"(qw[e][0]), "+v"(qw[e][1]), "+v"(qw[e][2]));
      }
      auto putq = [&](int m) {
        const int dt = m < 10 ? m / 5 : (m - 2) / 4, qt = m < 10 ? m % 5 : (m - 2) % 4;
        const u32x2 q = {cvtpk(acc[m][0] * QSCALE, acc[m][1] * QSCALE), cvtpk(acc[m][2] * QSCALE, acc[m][3] * QSCALE)};
        *(LAS u32x2*)((qt == 0 ? qw[dt >> 1][0] : qt == 4 ? qw[dt >> 1][2] : qw[dt >> 1][1] + qt * 4096) + (dt & 1) * 512) = q; GAP();
      };
#pragma unroll
      for (int m = 0; m < 18; ++m) {
        mm(1, m);
        if (m == 0) bar();
        if (m < 2) { a.K[2 * m] = lds128(a.ka[2 * m]); a.K[2 * m + 1] = lds128(a.ka[2 * m + 1]); GAP(); }
        if (m >= 3) putq(m - 3);
      }
      putq(15); putq(16); asm volatile("s_nop 1"); putq(17);
    }
    STAMP(1);
    waitvm<0>();
    asm volatile("" : "+v"(epoch) : "v"(tc));
    if (w == 0 && lane == 0) *(LAS unsigned long long*)(ldsb + TAGW) = epoch + 1ull;
    bar();
    STAMP(2);
    attend<5>(c, a, lane);
  }
  project(c, ((w + 2u) >> 2) & 1u);
}

extern "C" void kernel_launch(void* const* d_in, const int* in_sizes, int n_in, void* d_out, int out_size, void* d_ws, size_t ws_size, hipStream_t stream) {
  (void)hipFuncSetAttribute((const void*)fused_kernel, hipFuncAttributeMaxDynamicSharedMemorySize, LDS_BYTES);
  fused_kernel<<<256, 512, LDS_BYTES, stream>>>((const u16*)d_in[0], (const u16*)d_in[1], (const u16*)d_in[2], (const u16*)d_in[3], (const u16*)d_in[4], (char*)d_ws, (u16*)d_out);
}
```
